```python
import torch

def _softmax(x, axis=-1):
    return torch.softmax(x, dim=axis)

def _gelu(x):
    return torch.nn.functional.gelu(x, approximate='tanh')

def _silu(x):
    return torch.nn.functional.silu(x)

def _sigmoid(x):
    return torch.sigmoid(x)

def _layernorm(x, axis=-1, eps=1e-05):
    m = x.mean(dim=axis, keepdim=True)
    v = x.var(dim=axis, keepdim=True, unbiased=False)
    return (x - m) / torch.sqrt(v + eps)

def _scatter_add(tgt, idx, src, axis=0):
    return tgt.clone().index_add_(axis, idx, src)

def _segment_sum(src, seg, n):
    out = torch.zeros((n,) + src.shape[1:], dtype=src.dtype)
    return out.index_add_(0, seg, src)

def _pad(x, pw):
    flat = tuple((v for pair in reversed(pw) for v in pair))
    return torch.nn.functional.pad(x, flat)
kernel_name = 'interleave_layers__memory_aug_recurrent_mem_tr__geglu_ffn__prompt'

def setup_inputs():

    _w = lambda a: (lambda t: t.to(torch.bfloat16) if t.is_floating_point() else t)(torch.as_tensor(a))

    def _A_setup():
        B, S, Dm = (1, 2048, 512)
        H, Dh = (32, 128)
        M, seg = (8, 32)
        seg = min(seg, S)
        S = S // seg * seg
        return {'x': _w((lambda *s: torch.randn(*s, dtype=torch.bfloat16) * 0.05)(B, S, Dm)), 'mem0': _w((lambda *s: torch.randn(*s, dtype=torch.bfloat16) * 0.8)(B, M, Dm)), 'Wq': _w((lambda *s: torch.randn(*s, dtype=torch.bfloat16) * 0.8)(Dm, H, Dh) * Dm ** (-0.5)), 'Wk': _w((lambda *s: torch.randn(*s, dtype=torch.bfloat16) * 0.8)(Dm, H, Dh) * Dm ** (-0.5)), 'Wv': _w((lambda *s: torch.randn(*s, dtype=torch.bfloat16) * 0.8)(Dm, H, Dh) * Dm ** (-0.5)), 'Wo': _w((lambda *s: torch.randn(*s, dtype=torch.bfloat16) * 0.8)(H, Dh, Dm) * (H * Dh) ** (-0.5)), 'seg_len': seg}
    _A_in = _A_setup()

    def _B_setup():
        B, S, D, F = (1, 2048, 512, 1792)
        return {'x': _w((lambda *s: torch.randn(*s, dtype=torch.bfloat16) * 0.05)(B, S, D)), 'w_gate': _w((lambda *s: torch.randn(*s, dtype=torch.bfloat16) * 0.05)(D, F)), 'w_up': _w((lambda *s: torch.randn(*s, dtype=torch.bfloat16) * 0.05)(D, F)), 'w_down': _w((lambda *s: torch.randn(*s, dtype=torch.bfloat16) * 0.05)(F, D))}
    _B_in = _B_setup()
    return {'A_x': _A_in['x'], 'A_mem0': _A_in['mem0'], 'A_Wq': _A_in['Wq'], 'A_Wk': _A_in['Wk'], 'A_Wv': _A_in['Wv'], 'A_Wo': _A_in['Wo'], 'A_seg_len': _A_in['seg_len'], 'B_x': _B_in['x'], 'B_w_gate': _B_in['w_gate'], 'B_w_up': _B_in['w_up'], 'B_w_down': _B_in['w_down']}

def reference(A_x, A_mem0, A_Wq, A_Wk, A_Wv, A_Wo, A_seg_len, B_x, B_w_gate, B_w_up, B_w_down):

    def _A_ref(x, mem0, Wq, Wk, Wv, Wo, seg_len):
        B, S, Dm = x.shape
        H, Dh = (Wq.shape[1], Wq.shape[2])
        M = mem0.shape[1]
        scale = Dh ** (-0.5)
        n_seg = S // seg_len
        L = M + seg_len
        idx = torch.arange(L)
        mask = (idx[:, None] < M) | (idx[:, None] >= idx[None, :])
        mem = mem0
        outs = []
        for s in range(n_seg):
            xs = x[:, s * seg_len:(s + 1) * seg_len, :].clone()
            h = torch.concatenate([mem, xs], axis=1)
            q = torch.einsum('btd,dhe->bhte', h, Wq)
            k = torch.einsum('btd,dhe->bhte', h, Wk)
            v = torch.einsum('btd,dhe->bhte', h, Wv)
            scores = torch.einsum('bhqe,bhke->bhqk', q, k) * scale
            scores = torch.where(mask[None, None], scores, -1000000000.0)
            p = _softmax(scores, axis=-1)
            o = torch.einsum('bhqk,bhke->bhqe', p, v)
            o = torch.einsum('bhte,hed->btd', o, Wo)
            mem = o[:, :M, :].clone()
            outs.append(o[:, M:, :])
        return torch.concatenate(outs, axis=1)

    def _B_ref(x, w_gate, w_up, w_down):
        return _gelu(x @ w_gate) * (x @ w_up) @ w_down
    x = A_x
    x = x + _A_ref(_layernorm(x, axis=-1), A_mem0, A_Wq, A_Wk, A_Wv, A_Wo, A_seg_len)
    x = x + _B_ref(_layernorm(x, axis=-1), B_w_gate, B_w_up, B_w_down)
    x = x + _A_ref(_layernorm(x, axis=-1), A_mem0, A_Wq, A_Wk, A_Wv, A_Wo, A_seg_len)
    x = x + _B_ref(_layernorm(x, axis=-1), B_w_gate, B_w_up, B_w_down)
    return x
```

```cpp
#include <hip/hip_runtime.h>
#include <hip/hip_bf16.h>
#include <stdint.h>

#define GAS __attribute__((address_space(1)))
#define LAS __attribute__((address_space(3)))
typedef unsigned v4u __attribute__((ext_vector_type(4)));
typedef unsigned v2u __attribute__((ext_vector_type(2)));
typedef float f32x4 __attribute__((ext_vector_type(4)));
typedef float f32x2 __attribute__((ext_vector_type(2)));
typedef short bf16x8 __attribute__((ext_vector_type(8)));
typedef __bf16 bf16x2 __attribute__((ext_vector_type(2)));
typedef GAS unsigned gu32;
typedef unsigned short u16;
#define RLX_AGENT __ATOMIC_RELAXED, __HIP_MEMORY_SCOPE_AGENT
#define SB() __builtin_amdgcn_sched_barrier(0)
#define DRAIN() asm volatile("s_waitcnt vmcnt(0)" ::: "memory")
#define LDSWAIT() asm volatile("s_waitcnt lgkmcnt(0)" ::: "memory")

constexpr int S_TOK = 2048, DM = 512, NH = 32, DH = 128, NMEM = 8, SEG = 32, NSEG = 64, DFF = 1792;
constexpr int NFFN = 14, NATT = 16;

constexpr size_t KB = 1024, MB = 1024 * 1024;
constexpr size_t OFF_ABORT = 0;
constexpr size_t FLINE = 128;
constexpr size_t FBLK = 2 * NSEG * FLINE;
constexpr size_t OFF_F_QKV = 4 * KB;
constexpr size_t OFF_F_KM = OFF_F_QKV + FBLK;
constexpr size_t OFF_F_ATT = OFF_F_KM + FBLK;
constexpr size_t OFF_F_X1 = OFF_F_ATT + FBLK;
constexpr size_t OFF_F_FFN = OFF_F_X1 + FBLK;
constexpr size_t OFF_F_X2 = OFF_F_FFN + FBLK;
constexpr size_t OFF_P = 128 * KB;
constexpr size_t P_CHAIN = 64 * 32 * 256;
constexpr size_t ZERO_BYTES = OFF_P + 2 * P_CHAIN;
constexpr size_t OFF_M = ZERO_BYTES;
constexpr size_t M_SLOT = 8 * 8 * KB, M_CHAIN = 3 * M_SLOT;
constexpr size_t ONES_BYTES = 2 * M_CHAIN;
constexpr size_t OFF_WQKV = 2 * MB;
constexpr size_t OFF_WO = OFF_WQKV + 12 * MB;
constexpr size_t OFF_WG = OFF_WO + 4 * MB;
constexpr size_t OFF_WU = OFF_WG + NFFN * 128 * KB;
constexpr size_t OFF_WD = OFF_WU + NFFN * 128 * KB;
constexpr size_t OFF_XA = 24 * MB;
constexpr size_t OFF_XF = OFF_XA + 4 * MB;
constexpr size_t OFF_KVM = OFF_XF + 4 * MB;
constexpr size_t OFF_QKVF = OFF_KVM + 16 * MB;
constexpr size_t OFF_PO = OFF_QKVF + 96 * MB;
constexpr size_t OFF_PF = OFF_PO + 32 * MB;
constexpr size_t WS_BYTES = OFF_PF + 28 * MB;
static_assert(OFF_F_X2 + FBLK <= OFF_P, "flags");
static_assert(OFF_M + ONES_BYTES <= OFF_WQKV, "ctrl");
static_assert(OFF_WD + NFFN * 128 * KB <= OFF_XA, "weights");
static_assert(WS_BYTES <= 256 * MB, "workspace");

constexpr unsigned kSpinCap = 1u << 17;
constexpr int LDS_BYTES = 163840;

struct Params {
  const u16* x;
  const u16* mem0;
  u16* out;
  unsigned char* ws;
};
struct PrepParams {
  const u16* x;
  const u16* wq;
  const u16* wk;
  const u16* wv;
  const u16* wo;
  const u16* wg;
  const u16* wu;
  const u16* wd;
  unsigned char* ws;
};

__device__ __forceinline__ unsigned pack2(float a, float b) {
  f32x2 v = {a, b};
  bf16x2 r = __builtin_convertvector(v, bf16x2);
  return __builtin_bit_cast(unsigned, r);
}
__device__ __forceinline__ float bf16r(float a) { return __uint_as_float(pack2(a, 0.f) << 16); }
__device__ __forceinline__ float bflo(unsigned u) { return __uint_as_float(u << 16); }
__device__ __forceinline__ float bfhi(unsigned u) { return __uint_as_float(u & 0xffff0000u); }
#define MFMA "v_mfma_f32_16x16x32_bf16 "
__device__ __forceinline__ void mfma4a(f32x4& c0, f32x4& c1, f32x4& c2, f32x4& c3, v4u a0, v4u a1, v4u a2, v4u a3, v4u b) {
  asm("s_nop 1\n\t"
      "v_mfma_f32_16x16x32_bf16 %0, %4, %8, %0\n\t"
      "v_mfma_f32_16x16x32_bf16 %1, %5, %8, %1\n\t"
      "v_mfma_f32_16x16x32_bf16 %2, %6, %8, %2\n\t"
      "v_mfma_f32_16x16x32_bf16 %3, %7, %8, %3"
      : "+v"(c0), "+v"(c1), "+v"(c2), "+v"(c3)
      : "a"(a0), "a"(a1), "a"(a2), "a"(a3), "v"(b));
}
__device__ __forceinline__ void mfma6a(f32x4& c0, f32x4& c1, f32x4& c2, f32x4& c3, f32x4& c4, f32x4& c5,
                                       v4u a0, v4u a1, v4u a2, v4u a3, v4u b4, v4u b5, v4u b) {
  asm("s_nop 1\n\t"
      MFMA "%0, %6, %12, %0\n\t"
      MFMA "%1, %7, %12, %1\n\t"
      MFMA "%2, %8, %12, %2\n\t"
      MFMA "%3, %9, %12, %3\n\t"
      MFMA "%4, %12, %10, %4\n\t"
      MFMA "%5, %12, %11, %5"
      : "+v"(c0), "+v"(c1), "+v"(c2), "+v"(c3), "+v"(c4), "+v"(c5)
      : "a"(a0), "a"(a1), "a"(a2), "a"(a3), "v"(b4), "v"(b5), "v"(b));
}
__device__ __forceinline__ void mfma4v(f32x4& c0, f32x4& c1, f32x4& c2, f32x4& c3, v4u a0, v4u a1, v4u a2, v4u a3, v4u b) {
  asm("s_nop 1\n\t"
      MFMA "%0, %4, %8, %0\n\t"
      MFMA "%1, %5, %8, %1\n\t"
      MFMA "%2, %6, %8, %2\n\t"
      MFMA "%3, %7, %8, %3"
      : "+v"(c0), "+v"(c1), "+v"(c2), "+v"(c3)
      : "v"(a0), "v"(a1), "v"(a2), "v"(a3), "v"(b));
}
__device__ __forceinline__ void mfma3v(f32x4& c0, f32x4& c1, f32x4& c2, v4u a0, v4u a1, v4u a2, v4u b) {
  asm("s_nop 1\n\t"
      MFMA "%0, %3, %6, %0\n\t"
      MFMA "%1, %4, %6, %1\n\t"
      MFMA "%2, %5, %6, %2"
      : "+v"(c0), "+v"(c1), "+v"(c2)
      : "v"(a0), "v"(a1), "v"(a2), "v"(b));
}
__device__ __forceinline__ void mfma_pv(f32x4& c0, f32x4& c1, v4u a0, v4u a1, v4u m0, v4u m1, v4u p0, v4u p1) {
  asm("s_nop 1\n\t"
      MFMA "%0, %2, %6, 0\n\t"
      MFMA "%1, %3, %6, 0\n\t"
      MFMA "%0, %4, %7, %0\n\t"
      MFMA "%1, %5, %7, %1"
      : "=&v"(c0), "=&v"(c1)
      : "v"(a0), "v"(a1), "v"(m0), "v"(m1), "v"(p0), "v"(p1));
}
__device__ __forceinline__ void mfma_k4(f32x4& c, v4u a0, v4u a1, v4u a2, v4u a3, v4u b0, v4u b1, v4u b2, v4u b3) {
  asm("s_nop 1\n\t"
      MFMA "%0, %1, %5, 0\n\t"
      MFMA "%0, %2, %6, %0\n\t"
      MFMA "%0, %3, %7, %0\n\t"
      MFMA "%0, %4, %8, %0"
      : "=&v"(c)
      : "v"(a0), "v"(a1), "v"(a2), "v"(a3), "v"(b0), "v"(b1), "v"(b2), "v"(b3));
}
__device__ __forceinline__ void mfma_settle2(f32x4& c0, f32x4& c1) { asm("s_nop 7\n\ts_nop 1" : "+v"(c0), "+v"(c1)); }
__device__ __forceinline__ void mfma_settle3(f32x4& c0, f32x4& c1, f32x4& c2) { asm("s_nop 7\n\ts_nop 1" : "+v"(c0), "+v"(c1), "+v"(c2)); }
__device__ __forceinline__ void mfma_settle(f32x4& c0, f32x4& c1, f32x4& c2, f32x4& c3) {
  asm("s_nop 7\n\ts_nop 1" : "+v"(c0), "+v"(c1), "+v"(c2), "+v"(c3));
}
__device__ __forceinline__ v4u pack8(f32x4 a, f32x4 b) {
  v4u r;
  r.x = pack2(a.x, a.y); r.y = pack2(a.z, a.w); r.z = pack2(b.x, b.y); r.w = pack2(b.z, b.w);
  return r;
}
__device__ __forceinline__ float xmax(float x) {
  auto s = __builtin_amdgcn_permlane16_swap(__float_as_uint(x), __float_as_uint(x), false, false);
  x = fmaxf(__uint_as_float(s[0]), __uint_as_float(s[1]));
  auto t = __builtin_amdgcn_permlane32_swap(__float_as_uint(x), __float_as_uint(x), false, false);
  return fmaxf(__uint_as_float(t[0]), __uint_as_float(t[1]));
}
__device__ __forceinline__ float xsum(float x) {
  auto s = __builtin_amdgcn_permlane16_swap(__float_as_uint(x), __float_as_uint(x), false, false);
  x = __uint_as_float(s[0]) + __uint_as_float(s[1]);
  auto t = __builtin_amdgcn_permlane32_swap(__float_as_uint(x), __float_as_uint(x), false, false);
  return __uint_as_float(t[0]) + __uint_as_float(t[1]);
}
__device__ __forceinline__ float wave_sum(float x) {
  x += __shfl_xor(x, 1); x += __shfl_xor(x, 2); x += __shfl_xor(x, 4); x += __shfl_xor(x, 8);
  return xsum(x);
}
typedef __amdgpu_buffer_rsrc_t rsrc_t;
__device__ __forceinline__ v4u ld16(rsrc_t rs, unsigned off) { return __builtin_amdgcn_raw_buffer_load_b128(rs, (int)off, 0, 16); }
__device__ __forceinline__ v2u ld8(rsrc_t rs, unsigned off) { return __builtin_amdgcn_raw_buffer_load_b64(rs, (int)off, 0, 16); }
__device__ __forceinline__ unsigned ld4(rsrc_t rs, unsigned off) { return __builtin_amdgcn_raw_buffer_load_b32(rs, (int)off, 0, 16); }
__device__ __forceinline__ v4u ld16p(rsrc_t rs, unsigned off) { return __builtin_amdgcn_raw_buffer_load_b128(rs, (int)off, 0, 0); }
__device__ __forceinline__ void st16(rsrc_t rs, unsigned off, v4u v) { __builtin_amdgcn_raw_buffer_store_b128(v, rs, (int)off, 0, 16); }
__device__ __forceinline__ void st8(rsrc_t rs, unsigned off, v2u v) { __builtin_amdgcn_raw_buffer_store_b64(v, rs, (int)off, 0, 16); }
__device__ __forceinline__ void st4(rsrc_t rs, unsigned off, unsigned v) { __builtin_amdgcn_raw_buffer_store_b32(v, rs, (int)off, 0, 16); }

__device__ __forceinline__ bool spin_fail(unsigned& spins, rsrc_t rs) {
  ++spins;
  if (spins > kSpinCap) { st4(rs, (unsigned)OFF_ABORT, 1u); return true; }
  if ((spins & 127u) == 0u) return __builtin_amdgcn_readfirstlane(ld4(rs, (unsigned)OFF_ABORT)) != 0u;
  return false;
}
__device__ __forceinline__ void wait_flags(rsrc_t rs, unsigned off, int n, int lane) {
  unsigned spins = 0;
  for (;;) {
    const unsigned v = ld4(rs, off + (unsigned)(lane < n ? lane : 0) * 4u);
    if (__all(v != 0u)) break;
    if (spin_fail(spins, rs)) break;
    __builtin_amdgcn_s_sleep(1);
  }
  __builtin_amdgcn_fence(__ATOMIC_ACQUIRE, "wavefront");
}

__device__ __forceinline__ void layernorm8(const float (&x)[8], float (&y)[8]) {
  float s = 0.f;
#pragma unroll
  for (int j = 0; j < 8; ++j) s += x[j];
  const float mean = wave_sum(s) * (1.0f / DM);
  float q = 0.f;
#pragma unroll
  for (int j = 0; j < 8; ++j) { const float d = x[j] - mean; q += d * d; }
  const float var = wave_sum(q) * (1.0f / DM);
  const float m = bf16r(mean);
  const float den = bf16r(sqrtf(bf16r(bf16r(var) + 1e-5f)));
#pragma unroll
  for (int j = 0; j < 8; ++j) y[j] = bf16r(x[j] - m) / den;
}

constexpr int NJ_QKV = 3 * 16 * 32;
constexpr int NJ_WO = 32 * 4 * 4;
constexpr int NJ_GU = 2 * 16 * NFFN;
constexpr int NJ_WD = NFFN * 4 * 4;
constexpr int NJ_LN = S_TOK / 4;
constexpr int NJ_INIT = 64;
constexpr int NJ_ALL = NJ_QKV + NJ_WO + NJ_GU + NJ_WD + NJ_LN + NJ_INIT;

__global__ void __launch_bounds__(256) k_prep(PrepParams p) {
  __shared__ __attribute__((aligned(16))) u16 tile[32][136];
  const int tid = threadIdx.x, lane = tid & 63, w = tid >> 6;
  int job = blockIdx.x;
  if (job < NJ_QKV + NJ_WO + NJ_GU + NJ_WD) {
    const u16* src; int ld; bool pi; size_t dst; size_t tstride;
    if (job < NJ_QKV) {
      const int m = job / 512, ks = (job >> 5) & 15, h = job & 31;
      const u16* W = m == 0 ? p.wq : (m == 1 ? p.wk : p.wv);
      src = W + (size_t)(32 * ks) * (NH * DH) + h * DH; ld = NH * DH; pi = false;
      dst = OFF_WQKV + ((size_t)(m * 32 + h) * 8 * 16 + ks) * KB; tstride = 16 * KB;
    } else if (job < NJ_QKV + NJ_WO) {
      job -= NJ_QKV;
      const int h = job >> 4, ks = (job >> 2) & 3, nb = job & 3;
      src = p.wo + (size_t)(h * DH + 32 * ks) * DM + nb * 128; ld = DM; pi = true;
      dst = OFF_WO + ((size_t)(h * 32 + nb * 8) * 4 + ks) * KB; tstride = 4 * KB;
    } else if (job < NJ_QKV + NJ_WO + NJ_GU) {
      job -= NJ_QKV + NJ_WO;
      const int m = job / (16 * NFFN), r = job % (16 * NFFN), ks = r / NFFN, sl = r % NFFN;
      src = (m == 0 ? p.wg : p.wu) + (size_t)(32 * ks) * DFF + sl * 128; ld = DFF; pi = false;
      dst = (m == 0 ? OFF_WG : OFF_WU) + ((size_t)sl * 8 * 16 + ks) * KB; tstride = 16 * KB;
    } else {
      job -= NJ_QKV + NJ_WO + NJ_GU;
      const int sl = job >> 4, ks = (job >> 2) & 3, nb = job & 3;
      src = p.wd + (size_t)(sl * 128 + 32 * ks) * DM + nb * 128; ld = DM; pi = true;
      dst = OFF_WD + ((size_t)(sl * 32 + nb * 8) * 4 + ks) * KB; tstride = 4 * KB;
    }
#pragma unroll
    for (int it = 0; it < 2; ++it) {
      const int idx = it * 256 + tid, r = idx >> 4, c = idx & 15;
      const v4u v = *(const v4u*)(src + (size_t)r * ld + c * 8);
      *(v4u*)&tile[r][c * 8] = v;
    }
    __syncthreads();
    const int g = lane >> 4, r = lane & 15;
#pragma unroll
    for (int it = 0; it < 2; ++it) {
      const int t = w * 2 + it;
      unsigned e[8];
#pragma unroll
      for (int j = 0; j < 8; ++j) {
        const int k = pi ? (16 * (j >> 2) + 4 * g + (j & 3)) : (8 * g + j);
        e[j] = tile[k][16 * t + r];
      }
      v4u o; o.x = e[0] | (e[1] << 16); o.y = e[2] | (e[3] << 16); o.z = e[4] | (e[5] << 16); o.w = e[6] | (e[7] << 16);
      *(v4u*)(p.ws + dst + (size_t)t * tstride + lane * 16) = o;
    }
    return;
  }
  job -= NJ_QKV + NJ_WO + NJ_GU + NJ_WD;
  if (job < NJ_LN) {
    const int row = job * 4 + w;
    const v4u v = *(const v4u*)(p.x + (size_t)row * DM + lane * 8);
    float x[8] = {bflo(v.x), bfhi(v.x), bflo(v.y), bfhi(v.y), bflo(v.z), bfhi(v.z), bflo(v.w), bfhi(v.w)}, y[8];
    layernorm8(x, y);
    v4u o; o.x = pack2(y[0], y[1]); o.y = pack2(y[2], y[3]); o.z = pack2(y[4], y[5]); o.w = pack2(y[6], y[7]);
    *(v4u*)(p.ws + OFF_XA + (size_t)row * 1024 + lane * 16) = o;
    return;
  }
  job -= NJ_LN;
  {
    const v4u z = {0u, 0u, 0u, 0u}, o = {~0u, ~0u, ~0u, ~0u};
    for (size_t i = (size_t)job * 256 + tid; i < ZERO_BYTES / 16; i += (size_t)NJ_INIT * 256) *(v4u*)(p.ws + i * 16) = z;
    for (size_t i = (size_t)job * 256 + tid; i < ONES_BYTES / 16; i += (size_t)NJ_INIT * 256) *(v4u*)(p.ws + OFF_M + i * 16) = o;
  }
}

constexpr float kScale = 0.08838834764831845f;
constexpr float kNegBig = -3.0e38f;

__device__ __forceinline__ void softmax_frags(f32x4 sx0, f32x4 sx1, f32x4 sm, unsigned ok0, unsigned ok1, bool okm, v4u& p0, v4u& p1) {
  float a[12];
#pragma unroll
  for (int r = 0; r < 4; ++r) {
    a[r] = ((ok0 >> r) & 1u) ? bf16r(bf16r(sx0[r]) * kScale) : kNegBig;
    a[4 + r] = ((ok1 >> r) & 1u) ? bf16r(bf16r(sx1[r]) * kScale) : kNegBig;
    a[8 + r] = okm ? bf16r(bf16r(sm[r]) * kScale) : kNegBig;
  }
  float mx = a[0];
#pragma unroll
  for (int r = 1; r < 12; ++r) mx = fmaxf(mx, a[r]);
  mx = xmax(mx);
  float sum = 0.f;
#pragma unroll
  for (int r = 0; r < 4; ++r) {
    a[r] = ((ok0 >> r) & 1u) ? __expf(a[r] - mx) : 0.f;
    a[4 + r] = ((ok1 >> r) & 1u) ? __expf(a[4 + r] - mx) : 0.f;
    a[8 + r] = okm ? __expf(a[8 + r] - mx) : 0.f;
  }
#pragma unroll
  for (int r = 0; r < 12; ++r) sum += a[r];
  sum = xsum(sum);
  const float inv = 1.0f / sum;
  p0.x = pack2(a[0] * inv, a[1] * inv); p0.y = pack2(a[2] * inv, a[3] * inv);
  p0.z = pack2(a[4] * inv, a[5] * inv); p0.w = pack2(a[6] * inv, a[7] * inv);
  p1.x = pack2(a[8] * inv, a[9] * inv); p1.y = pack2(a[10] * inv, a[11] * inv);
  p1.z = 0u; p1.w = 0u;
}

__device__ __forceinline__ void stage_rows(rsrc_t rs, unsigned goff, LAS unsigned char* L, int w, int lane) {
  v4u v[8];
#pragma unroll
  for (int i = 0; i < 8; ++i) v[i] = ld16(rs, goff + (unsigned)(8 * w + i) * 1024u + (unsigned)lane * 16u);
  SB();
#pragma unroll
  for (int i = 0; i < 8; ++i) {
    const int row = 8 * w + i;
    *(LAS v4u*)(L + row * 1024 + ((lane ^ (row & 15)) * 16)) = v[i];
  }
}
__device__ __forceinline__ v4u row_frag(const LAS unsigned char* L, int tt, int ks, int g, int r) {
  return *(const LAS v4u*)(L + (16 * tt + r) * 1024 + (((4 * ks + g) ^ r) * 16));
}
__device__ __forceinline__ void store_partial(rsrc_t rs, unsigned goff, LAS unsigned char* L, const f32x4 (&acc)[8][2], int w, int lane) {
  const int g = lane >> 4, i = lane & 15;
#pragma unroll
  for (int dt = 0; dt < 8; ++dt)
#pragma unroll
    for (int tt = 0; tt < 2; ++tt) {
      v2u o; o.x = pack2(acc[dt][tt].x, acc[dt][tt].y); o.y = pack2(acc[dt][tt].z, acc[dt][tt].w);
      const int c = 2 * (8 * w + dt) + (g >> 1);
      *(LAS v2u*)(L + (16 * tt + i) * 1024 + ((c ^ i) * 16) + (g & 1) * 8) = o;
    }
  __syncthreads();
#pragma unroll
  for (int k = 0; k < 8; ++k) {
    const int row = 8 * w + k;
    const v4u v = *(const LAS v4u*)(L + row * 1024 + ((lane ^ (row & 15)) * 16));
    st16(rs, goff + (unsigned)row * 1024u + (unsigned)lane * 16u, v);
  }
}

constexpr int CH_WO = 0, CH_KV = 131072, CH_MEM = CH_KV + 16384, CH_EX = CH_MEM + 8192;
__device__ __forceinline__ void role_chain(const Params& p, rsrc_t rs, LAS unsigned char* L, int c, int h, int copy) {
  const int tid = threadIdx.x, lane = tid & 63, w = __builtin_amdgcn_readfirstlane(tid >> 6);
  const int g = lane >> 4, r = lane & 15, r8 = lane & 7;
  v4u wq[2][16], wk[2][16], wv[2][16];
#pragma unroll
  for (int t = 0; t < 2; ++t)
#pragma unroll
    for (int ks = 0; ks < 16; ++ks) {
      const unsigned o = (unsigned)(((h * 8 + 2 * w + t) * 16 + ks) * 1024 + lane * 16);
      wq[t][ks] = ld16p(rs, (unsigned)OFF_WQKV + o);
      wk[t][ks] = ld16p(rs, (unsigned)(OFF_WQKV + 4 * MB) + o);
      wv[t][ks] = ld16p(rs, (unsigned)(OFF_WQKV + 8 * MB) + o);
    }
  for (int i = 0; i < 32; ++i) {
    const unsigned o = (unsigned)((i * 4 + w) * 1024 + lane * 16);
    *(LAS v4u*)(L + CH_WO + o) = ld16p(rs, (unsigned)(OFF_WO + (size_t)h * 128 * KB) + o);
  }
  {
#pragma unroll
    for (int it = 0; it < 2; ++it) {
      const int idx = it * 256 + tid, i = idx >> 6, ch = idx & 63;
      const v4u v = *(const v4u*)(p.mem0 + i * DM + ch * 8);
      *(LAS v4u*)(L + CH_MEM + (ch >> 2) * 512 + (ch & 3) * 128 + i * 16) = v;
    }
  }
  const unsigned qkv_base = (unsigned)(OFF_QKVF + (size_t)c * NSEG * NH * 24 * KB) + (unsigned)h * 24u * 1024u + 8192u;
  const unsigned kvm_base = (unsigned)(OFF_KVM + (size_t)c * NSEG * NH * 4 * KB) + (unsigned)h * 4096u;
  const unsigned p_base = (unsigned)(OFF_P + (size_t)c * P_CHAIN) + (unsigned)h * 256u;
  const unsigned m_base = (unsigned)(OFF_M + (size_t)c * M_CHAIN) + (unsigned)copy * 8192u;
  {
    wait_flags(rs, (unsigned)(OFF_F_QKV + (size_t)(c * NSEG) * FLINE) + (unsigned)h * 4u, 1, lane);
    v4u v[4];
#pragma unroll
    for (int i = 0; i < 4; ++i) v[i] = ld16(rs, qkv_base + (unsigned)((4 * w + i) * 1024 + lane * 16));
    SB();
#pragma unroll
    for (int i = 0; i < 4; ++i) *(LAS v4u*)(L + CH_KV + (4 * w + i) * 1024 + lane * 16) = v[i];
  }
  __syncthreads();
  for (int s = 0; s < NSEG; ++s) {
    f32x4 aq[2], ak[2], av[2];
#pragma unroll
    for (int t = 0; t < 2; ++t) { aq[t] = f32x4{0.f, 0.f, 0.f, 0.f}; ak[t] = aq[t]; av[t] = aq[t]; }
#pragma unroll
    for (int ks = 0; ks < 16; ++ks) {
      const v4u mf = *(const LAS v4u*)(L + CH_MEM + ks * 512 + g * 128 + r8 * 16);
      mfma6a(aq[0], aq[1], ak[0], ak[1], av[0], av[1], wq[0][ks], wq[1][ks], wk[0][ks], wk[1][ks], wv[0][ks], wv[1][ks], mf);
    }
    mfma_settle(aq[0], aq[1], ak[0], ak[1]);
    mfma_settle2(av[0], av[1]);
    if (r < 8) {
      *(LAS v4u*)(L + CH_EX + w * 512 + g * 128 + r * 16) = pack8(ak[0], ak[1]);
      *(LAS v4u*)(L + CH_EX + 4096 + w * 512 + g * 128 + r * 16) = pack8(aq[0], aq[1]);
    }
    if (g < 2) {
#pragma unroll
      for (int t = 0; t < 2; ++t) {
        v2u o; o.x = pack2(av[t].x, av[t].y); o.y = pack2(av[t].z, av[t].w);
        *(LAS v2u*)(L + CH_EX + 2048 + (2 * w + t) * 256 + g * 128 + r * 8) = o;
      }
    }
    __syncthreads();
    f32x4 sx0 = {0.f, 0.f, 0.f, 0.f}, sx1 = sx0, sm = sx0;
#pragma unroll
    for (int ks = 0; ks < 4; ++ks) {
      const v4u qf = *(const LAS v4u*)(L + CH_EX + 4096 + ks * 512 + g * 128 + r8 * 16);
      const v4u km = *(const LAS v4u*)(L + CH_EX + ks * 512 + g * 128 + r8 * 16);
      const v4u k0 = *(const LAS v4u*)(L + CH_KV + ks * 1024 + lane * 16);
      const v4u k1 = *(const LAS v4u*)(L + CH_KV + (4 + ks) * 1024 + lane * 16);
      mfma3v(sx0, sx1, sm, k0, k1, km, qf);
    }
    mfma_settle3(sx0, sx1, sm);
    v4u p0, p1;
    softmax_frags(sx0, sx1, sm, 15u, 15u, g < 2, p0, p1);
    v4u of[4];
    {
      f32x4 o2[8];
#pragma unroll
      for (int m = 0; m < 4; ++m) {
        v4u vx[2], vm[2];
#pragma unroll
        for (int t = 0; t < 2; ++t) {
          const int et = 2 * m + t;
          vx[t] = *(const LAS v4u*)(L + CH_KV + 8192 + et * 1024 + lane * 16);
          const v2u vm2 = *(const LAS v2u*)(L + CH_EX + 2048 + et * 256 + (g & 1) * 128 + r * 8);
          vm[t].x = g < 2 ? vm2.x : 0u; vm[t].y = g < 2 ? vm2.y : 0u; vm[t].z = 0u; vm[t].w = 0u;
        }
        mfma_pv(o2[2 * m], o2[2 * m + 1], vx[0], vx[1], vm[0], vm[1], p0, p1);
      }
      mfma_settle(o2[0], o2[1], o2[2], o2[3]);
      mfma_settle(o2[4], o2[5], o2[6], o2[7]);
#pragma unroll
      for (int m = 0; m < 4; ++m) of[m] = pack8(o2[2 * m], o2[2 * m + 1]);
    }
    const unsigned tag = (unsigned)(s + 1) & 3u;
    if (s + 1 < NSEG) {
      f32x4 pa[8];
#pragma unroll
      for (int dt = 0; dt < 8; ++dt) {
        v4u wf[4];
#pragma unroll
        for (int ks = 0; ks < 4; ++ks) wf[ks] = *(const LAS v4u*)(L + CH_WO + ((8 * w + dt) * 4 + ks) * 1024 + lane * 16);
        mfma_k4(pa[dt], wf[0], wf[1], wf[2], wf[3], of[0], of[1], of[2], of[3]);
      }
#pragma unroll
      for (int hf = 0; hf < 2; ++hf) {
        mfma_settle(pa[4 * hf], pa[4 * hf + 1], pa[4 * hf + 2], pa[4 * hf + 3]);
#pragma unroll
        for (int d4 = 0; d4 < 4; ++d4) {
          const int dt = 4 * hf + d4;
          const f32x4 a = pa[dt];
          v4u o;
          o.x = (__float_as_uint(a.x) & ~3u) | tag; o.y = (__float_as_uint(a.y) & ~3u) | tag;
          o.z = (__float_as_uint(a.z) & ~3u) | tag; o.w = (__float_as_uint(a.w) & ~3u) | tag;
          const int v = 2 * (8 * w + dt) + (g >> 1);
          if (r < 8) st16(rs, p_base + (unsigned)(v * 8192 + (g & 1) * 128 + r * 16), o);
        }
      }
    }
    {
      const v4u v = *(const LAS v4u*)(L + CH_EX + w * 1024 + lane * 16);
      st16(rs, kvm_base + (unsigned)(s * NH * 4096 + w * 1024 + lane * 16), v);
      DRAIN();
    }
    __syncthreads();
    if (tid == 0) st4(rs, (unsigned)(OFF_F_KM + (size_t)(c * NSEG + s) * FLINE) + (unsigned)h * 4u, 1u);
    if (s + 1 < NSEG) {
      wait_flags(rs, (unsigned)(OFF_F_QKV + (size_t)(c * NSEG + s + 1) * FLINE) + (unsigned)h * 4u, 1, lane);
      v4u v[4];
#pragma unroll
      for (int i = 0; i < 4; ++i) v[i] = ld16(rs, qkv_base + (unsigned)((s + 1) * NH * 24 * 1024 + (4 * w + i) * 1024 + lane * 16));
      SB();
#pragma unroll
      for (int i = 0; i < 4; ++i) *(LAS v4u*)(L + CH_KV + (4 * w + i) * 1024 + lane * 16) = v[i];
      const unsigned mo = m_base + (unsigned)(((s + 1) % 3) * M_SLOT) + (unsigned)(w * 2048 + lane * 16);
      v4u x0, x1; unsigned spins = 0;
      for (;;) {
        x0 = ld16(rs, mo); x1 = ld16(rs, mo + 1024u);
        SB();
        const unsigned mx = max(max(max(x0.x, x0.y), max(x0.z, x0.w)), max(max(x1.x, x1.y), max(x1.z, x1.w)));
        if (__all(mx != 0xffffffffu)) break;
        if (spin_fail(spins, rs)) break;
      }
      *(LAS v4u*)(L + CH_MEM + w * 2048 + lane * 16) = x0;
      *(LAS v4u*)(L + CH_MEM + w * 2048 + 1024 + lane * 16) = x1;
      __syncthreads();
    }
  }
}

__device__ __forceinline__ void role_creduce(rsrc_t rs, int c, int idx) {
  const int tid = threadIdx.x, lane = tid & 63, w = __builtin_amdgcn_readfirstlane(tid >> 6);
  const int v = idx * 4 + w;
  const unsigned p_base = (unsigned)(OFF_P + (size_t)c * P_CHAIN) + (unsigned)(v * 8192 + lane * 16);
  const unsigned m_off = (unsigned)(OFF_M + (size_t)c * M_CHAIN) + (unsigned)((lane >> 4) * 8192 + (v >> 2) * 512 + (v & 3) * 128 + (lane & 7) * 16 + ((lane >> 3) & 1) * 8);
  for (int s = 0; s + 1 < NSEG; ++s) {
    const unsigned tag = (unsigned)(s + 1) & 3u;
    v4u x[8]; unsigned spins = 0;
    for (;;) {
#pragma unroll
      for (int k = 0; k < 8; ++k) x[k] = ld16(rs, p_base + (unsigned)k * 1024u);
      SB();
      unsigned bad = 0;
#pragma unroll
      for (int k = 0; k < 8; ++k) bad |= (x[k].x ^ tag) | (x[k].y ^ tag) | (x[k].z ^ tag) | (x[k].w ^ tag);
      if (__all((bad & 3u) == 0u)) break;
      if (spin_fail(spins, rs)) break;
    }
    float a0 = 0.f, a1 = 0.f, a2 = 0.f, a3 = 0.f;
#pragma unroll
    for (int k = 0; k < 8; ++k) {
      a0 += __uint_as_float(x[k].x & ~3u); a1 += __uint_as_float(x[k].y & ~3u);
      a2 += __uint_as_float(x[k].z & ~3u); a3 += __uint_as_float(x[k].w & ~3u);
    }
    a0 = xsum(a0); a1 = xsum(a1); a2 = xsum(a2); a3 = xsum(a3);
    v2u o; o.x = pack2(a0, a1); o.y = pack2(a2, a3);
    if (o.x == 0xffffffffu) o.x = 0xffc0ffc0u;
    if (o.y == 0xffffffffu) o.y = 0xffc0ffc0u;
    const unsigned slot = (unsigned)(((s + 1) % 3) * M_SLOT);
    st8(rs, m_off + slot, o);
    st8(rs, m_off + slot + 4u * 8192u, o);
    const unsigned old = (unsigned)((s % 3) * M_SLOT);
    const v2u ones = {~0u, ~0u};
    st8(rs, m_off + old, ones);
    st8(rs, m_off + old + 4u * 8192u, ones);
  }
}

__device__ __forceinline__ void role_qkv(rsrc_t rs, LAS unsigned char* L, int c, int h) {
  const int tid = threadIdx.x, lane = tid & 63, w = __builtin_amdgcn_readfirstlane(tid >> 6);
  const int g = lane >> 4, r = lane & 15;
  v4u wq[2][16], wk[2][16], wv[2][16];
#pragma unroll
  for (int t = 0; t < 2; ++t)
#pragma unroll
    for (int ks = 0; ks < 16; ++ks) {
      const unsigned o = (unsigned)(((h * 8 + 2 * w + t) * 16 + ks) * 1024 + lane * 16);
      wq[t][ks] = ld16p(rs, (unsigned)OFF_WQKV + o);
      wk[t][ks] = ld16p(rs, (unsigned)(OFF_WQKV + 4 * MB) + o);
      wv[t][ks] = ld16p(rs, (unsigned)(OFF_WQKV + 8 * MB) + o);
    }
  for (int s = 0; s < NSEG; ++s) {
    LAS unsigned char* X = L + (s & 1) * 32768;
    if (c == 1) wait_flags(rs, (unsigned)(OFF_F_X2 + (size_t)s * FLINE), 32, lane);
    stage_rows(rs, (unsigned)(OFF_XA + (size_t)c * 2 * MB) + (unsigned)s * 32768u, X, w, lane);
    __syncthreads();
    f32x4 aq[2][2], ak[2][2], av[2][2];
#pragma unroll
    for (int t = 0; t < 2; ++t)
#pragma unroll
      for (int tt = 0; tt < 2; ++tt) { aq[t][tt] = f32x4{0.f, 0.f, 0.f, 0.f}; ak[t][tt] = aq[t][tt]; av[t][tt] = aq[t][tt]; }
#pragma unroll
    for (int ks = 0; ks < 16; ++ks)
#pragma unroll
      for (int tt = 0; tt < 2; ++tt) {
        const v4u xf = row_frag(X, tt, ks, g, r);
        mfma6a(aq[0][tt], aq[1][tt], ak[0][tt], ak[1][tt], av[0][tt], av[1][tt], wq[0][ks], wq[1][ks], wk[0][ks], wk[1][ks], wv[0][ks], wv[1][ks], xf);
      }
    mfma_settle(av[0][0], av[1][0], av[0][1], av[1][1]);
    mfma_settle(aq[0][0], aq[1][0], ak[0][0], ak[1][0]);
    mfma_settle(aq[0][1], aq[1][1], ak[0][1], ak[1][1]);
    const unsigned ob = (unsigned)(OFF_QKVF + (size_t)((c * NSEG + s) * NH + h) * 24 * KB) + (unsigned)lane * 16u;
#pragma unroll
    for (int tt = 0; tt < 2; ++tt) {
      st16(rs, ob + (unsigned)(8192 + (tt * 4 + w) * 1024), pack8(ak[0][tt], ak[1][tt]));
      st16(rs, ob + (unsigned)((tt * 4 + w) * 1024), pack8(aq[0][tt], aq[1][tt]));
    }
#pragma unroll
    for (int t = 0; t < 2; ++t) st16(rs, ob + (unsigned)(16384 + (2 * w + t) * 1024), pack8(av[t][0], av[t][1]));
    DRAIN();
    __syncthreads();
    if (tid == 0) st4(rs, (unsigned)(OFF_F_QKV + (size_t)(c * NSEG + s) * FLINE) + (unsigned)h * 4u, 1u);
  }
}

__device__ __forceinline__ void role_att(rsrc_t rs, LAS unsigned char* L, int c, int pp) {
  const int tid = threadIdx.x, lane = tid & 63, w = __builtin_amdgcn_readfirstlane(tid >> 6);
  const int g = lane >> 4, r = lane & 15, r8 = lane & 7;
  v4u wo[2][8][4];
#pragma unroll
  for (int hh = 0; hh < 2; ++hh)
#pragma unroll
    for (int dt = 0; dt < 8; ++dt)
#pragma unroll
      for (int ks = 0; ks < 4; ++ks)
        wo[hh][dt][ks] = ld16p(rs, (unsigned)OFF_WO + (unsigned)((((2 * pp + hh) * 32 + 8 * w + dt) * 4 + ks) * 1024 + lane * 16));
  const int hh = w >> 1, tt = w & 1, h = 2 * pp + hh;
  const int q = 16 * tt + r;
  unsigned ok0 = 0, ok1 = 0;
#pragma unroll
  for (int j = 0; j < 4; ++j) { ok0 |= (4 * g + j <= q ? 1u : 0u) << j; ok1 |= (16 + 4 * g + j <= q ? 1u : 0u) << j; }
  LAS unsigned char* EX = L;
  LAS unsigned char* TR = L + 16384;
  for (int s = 0; s < NSEG; ++s) {
    wait_flags(rs, (unsigned)(OFF_F_QKV + (size_t)(c * NSEG + s) * FLINE) + (unsigned)h * 4u, 1, lane);
    const unsigned ib = (unsigned)(OFF_QKVF + (size_t)((c * NSEG + s) * NH + h) * 24 * KB) + (unsigned)lane * 16u;
    v4u qf[4], kf[8], vf[8];
#pragma unroll
    for (int ks = 0; ks < 4; ++ks) qf[ks] = ld16(rs, ib + (unsigned)((tt * 4 + ks) * 1024));
#pragma unroll
    for (int i = 0; i < 8; ++i) kf[i] = ld16(rs, ib + (unsigned)(8192 + i * 1024));
#pragma unroll
    for (int i = 0; i < 8; ++i) vf[i] = ld16(rs, ib + (unsigned)(16384 + i * 1024));
    SB();
    wait_flags(rs, (unsigned)(OFF_F_KM + (size_t)(c * NSEG + s) * FLINE) + (unsigned)h * 4u, 1, lane);
    const unsigned mb = (unsigned)(OFF_KVM + (size_t)((c * NSEG + s) * NH + h) * 4 * KB);
    v4u km[4]; v2u vm2[8];
#pragma unroll
    for (int ks = 0; ks < 4; ++ks) km[ks] = ld16(rs, mb + (unsigned)(ks * 512 + g * 128 + r8 * 16));
#pragma unroll
    for (int et = 0; et < 8; ++et) vm2[et] = ld8(rs, mb + (unsigned)(2048 + et * 256 + (g & 1) * 128 + r * 8));
    SB();
    f32x4 sx0 = {0.f, 0.f, 0.f, 0.f}, sx1 = sx0, sm = sx0;
#pragma unroll
    for (int ks = 0; ks < 4; ++ks) mfma3v(sx0, sx1, sm, kf[ks], kf[4 + ks], km[ks], qf[ks]);
    mfma_settle3(sx0, sx1, sm);
    v4u p0, p1;
    softmax_frags(sx0, sx1, sm, ok0, ok1, g < 2, p0, p1);
    {
      f32x4 o2[8];
#pragma unroll
      for (int m = 0; m < 4; ++m) {
        v4u vm[2];
#pragma unroll
        for (int t = 0; t < 2; ++t) {
          const int et = 2 * m + t;
          vm[t].x = g < 2 ? vm2[et].x : 0u; vm[t].y = g < 2 ? vm2[et].y : 0u; vm[t].z = 0u; vm[t].w = 0u;
        }
        mfma_pv(o2[2 * m], o2[2 * m + 1], vf[2 * m], vf[2 * m + 1], vm[0], vm[1], p0, p1);
      }
      mfma_settle(o2[0], o2[1], o2[2], o2[3]);
      mfma_settle(o2[4], o2[5], o2[6], o2[7]);
#pragma unroll
      for (int m = 0; m < 4; ++m) *(LAS v4u*)(EX + (w * 4 + m) * 1024 + lane * 16) = pack8(o2[2 * m], o2[2 * m + 1]);
    }
    __syncthreads();
    f32x4 acc[8][2];
#pragma unroll
    for (int dt = 0; dt < 8; ++dt) { acc[dt][0] = f32x4{0.f, 0.f, 0.f, 0.f}; acc[dt][1] = acc[dt][0]; }
#pragma unroll
    for (int h2 = 0; h2 < 2; ++h2)
#pragma unroll
      for (int ks = 0; ks < 4; ++ks)
#pragma unroll
        for (int t2 = 0; t2 < 2; ++t2) {
          const v4u of = *(const LAS v4u*)(EX + ((h2 * 2 + t2) * 4 + ks) * 1024 + lane * 16);
          mfma4a(acc[0][t2], acc[1][t2], acc[2][t2], acc[3][t2], wo[h2][0][ks], wo[h2][1][ks], wo[h2][2][ks], wo[h2][3][ks], of);
          mfma4a(acc[4][t2], acc[5][t2], acc[6][t2], acc[7][t2], wo[h2][4][ks], wo[h2][5][ks], wo[h2][6][ks], wo[h2][7][ks], of);
        }
#pragma unroll
    for (int t2 = 0; t2 < 2; ++t2) {
      mfma_settle(acc[0][t2], acc[1][t2], acc[2][t2], acc[3][t2]);
      mfma_settle(acc[4][t2], acc[5][t2], acc[6][t2], acc[7][t2]);
    }
    store_partial(rs, (unsigned)(OFF_PO + (size_t)(s * NATT + pp) * 32 * KB), TR, acc, w, lane);
    DRAIN();
    __syncthreads();
    if (tid == 0) st4(rs, (unsigned)(OFF_F_ATT + (size_t)(c * NSEG + s) * FLINE) + (unsigned)pp * 4u, 1u);
  }
}

__device__ __forceinline__ float gelu_tanh(float x) {
  const float u = 0.7978845608028654f * (x + 0.044715f * x * x * x);
  const float e = __expf(2.0f * u);
  const float t = 1.0f - 2.0f / (e + 1.0f);
  return 0.5f * x * (1.0f + t);
}
__device__ __forceinline__ void role_ffn(rsrc_t rs, LAS unsigned char* L, int c, int sl) {
  const int tid = threadIdx.x, lane = tid & 63, w = __builtin_amdgcn_readfirstlane(tid >> 6);
  const int g = lane >> 4, r = lane & 15;
  v4u wg[2][16], wu[2][16], wd[8][4];
#pragma unroll
  for (int t = 0; t < 2; ++t)
#pragma unroll
    for (int ks = 0; ks < 16; ++ks) {
      const unsigned o = (unsigned)(((sl * 8 + 2 * w + t) * 16 + ks) * 1024 + lane * 16);
      wg[t][ks] = ld16p(rs, (unsigned)OFF_WG + o);
      wu[t][ks] = ld16p(rs, (unsigned)OFF_WU + o);
    }
#pragma unroll
  for (int dt = 0; dt < 8; ++dt)
#pragma unroll
    for (int ks = 0; ks < 4; ++ks) wd[dt][ks] = ld16p(rs, (unsigned)OFF_WD + (unsigned)(((sl * 32 + 8 * w + dt) * 4 + ks) * 1024 + lane * 16));
  LAS unsigned char* X = L;
  LAS unsigned char* EX = L + 32768;
  LAS unsigned char* TR = L + 40960;
  for (int s = 0; s < NSEG; ++s) {
    wait_flags(rs, (unsigned)(OFF_F_X1 + (size_t)(c * NSEG + s) * FLINE), 32, lane);
    stage_rows(rs, (unsigned)(OFF_XF + (size_t)c * 2 * MB) + (unsigned)s * 32768u, X, w, lane);
    __syncthreads();
    f32x4 ag[2][2], au[2][2];
#pragma unroll
    for (int t = 0; t < 2; ++t)
#pragma unroll
      for (int tt = 0; tt < 2; ++tt) { ag[t][tt] = f32x4{0.f, 0.f, 0.f, 0.f}; au[t][tt] = ag[t][tt]; }
#pragma unroll
    for (int ks = 0; ks < 16; ++ks)
#pragma unroll
      for (int tt = 0; tt < 2; ++tt) {
        const v4u xf = row_frag(X, tt, ks, g, r);
        mfma4a(ag[0][tt], ag[1][tt], au[0][tt], au[1][tt], wg[0][ks], wg[1][ks], wu[0][ks], wu[1][ks], xf);
      }
    mfma_settle(ag[0][0], ag[1][0], au[0][0], au[1][0]);
    mfma_settle(ag[0][1], ag[1][1], au[0][1], au[1][1]);
#pragma unroll
    for (int tt = 0; tt < 2; ++tt) {
      f32x4 hv[2];
#pragma unroll
      for (int t = 0; t < 2; ++t)
#pragma unroll
        for (int j = 0; j < 4; ++j) hv[t][j] = bf16r(gelu_tanh(bf16r(ag[t][tt][j]))) * bf16r(au[t][tt][j]);
      *(LAS v4u*)(EX + (tt * 4 + w) * 1024 + lane * 16) = pack8(hv[0], hv[1]);
    }
    __syncthreads();
    f32x4 acc[8][2];
#pragma unroll
    for (int dt = 0; dt < 8; ++dt) { acc[dt][0] = f32x4{0.f, 0.f, 0.f, 0.f}; acc[dt][1] = acc[dt][0]; }
#pragma unroll
    for (int ks = 0; ks < 4; ++ks)
#pragma unroll
      for (int tt = 0; tt < 2; ++tt) {
        const v4u hf = *(const LAS v4u*)(EX + (tt * 4 + ks) * 1024 + lane * 16);
        mfma4v(acc[0][tt], acc[1][tt], acc[2][tt], acc[3][tt], wd[0][ks], wd[1][ks], wd[2][ks], wd[3][ks], hf);
        mfma4v(acc[4][tt], acc[5][tt], acc[6][tt], acc[7][tt], wd[4][ks], wd[5][ks], wd[6][ks], wd[7][ks], hf);
      }
#pragma unroll
    for (int t2 = 0; t2 < 2; ++t2) {
      mfma_settle(acc[0][t2], acc[1][t2], acc[2][t2], acc[3][t2]);
      mfma_settle(acc[4][t2], acc[5][t2], acc[6][t2], acc[7][t2]);
    }
    store_partial(rs, (unsigned)(OFF_PF + (size_t)(s * NFFN + sl) * 32 * KB), TR, acc, w, lane);
    DRAIN();
    __syncthreads();
    if (tid == 0) st4(rs, (unsigned)(OFF_F_FFN + (size_t)(c * NSEG + s) * FLINE) + (unsigned)sl * 4u, 1u);
  }
}

template <int NP>
__device__ __forceinline__ void sum_rows(rsrc_t rs, unsigned off, float (&a)[8]) {
  v4u v[NP];
#pragma unroll
  for (int i = 0; i < NP; ++i) v[i] = ld16(rs, off + (unsigned)i * 32768u);
  SB();
#pragma unroll
  for (int j = 0; j < 8; ++j) a[j] = 0.f;
#pragma unroll
  for (int i = 0; i < NP; ++i) {
    a[0] += bflo(v[i].x); a[1] += bfhi(v[i].x); a[2] += bflo(v[i].y); a[3] += bfhi(v[i].y);
    a[4] += bflo(v[i].z); a[5] += bfhi(v[i].z); a[6] += bflo(v[i].w); a[7] += bfhi(v[i].w);
  }
}
__device__ __forceinline__ void role_red(const Params& p, rsrc_t rs, LAS unsigned char* L, int t) {
  const int tid = threadIdx.x, lane = tid & 63, w = __builtin_amdgcn_readfirstlane(tid >> 6);
  const int c = w >> 1, ffn = w & 1;
  for (int s = 0; s < NSEG; ++s) {
    float a[8];
    if (!ffn) {
      wait_flags(rs, (unsigned)(OFF_F_ATT + (size_t)(c * NSEG + s) * FLINE), NATT, lane);
      sum_rows<NATT>(rs, (unsigned)(OFF_PO + (size_t)s * NATT * 32 * KB) + (unsigned)(t * 1024 + lane * 16), a);
    } else {
      wait_flags(rs, (unsigned)(OFF_F_FFN + (size_t)(c * NSEG + s) * FLINE), NFFN, lane);
      sum_rows<NFFN>(rs, (unsigned)(OFF_PF + (size_t)s * NFFN * 32 * KB) + (unsigned)(t * 1024 + lane * 16), a);
    }
    v4u xo;
    if (w == 0) xo = *(const v4u*)(p.x + (size_t)(s * SEG + t) * DM + lane * 8);
    else xo = *(volatile LAS v4u*)(L + s * 1024 + lane * 16);
    float x[8] = {bflo(xo.x), bfhi(xo.x), bflo(xo.y), bfhi(xo.y), bflo(xo.z), bfhi(xo.z), bflo(xo.w), bfhi(xo.w)};
#pragma unroll
    for (int j = 0; j < 8; ++j) x[j] = bf16r(x[j] + bf16r(a[j]));
    v4u xn; xn.x = pack2(x[0], x[1]); xn.y = pack2(x[2], x[3]); xn.z = pack2(x[4], x[5]); xn.w = pack2(x[6], x[7]);
    if (w == 3) {
      *(v4u*)(p.out + (size_t)(s * SEG + t) * DM + lane * 8) = xn;
    } else {
      *(volatile LAS v4u*)(L + s * 1024 + lane * 16) = xn;
      float y[8];
      layernorm8(x, y);
      v4u o; o.x = pack2(y[0], y[1]); o.y = pack2(y[2], y[3]); o.z = pack2(y[4], y[5]); o.w = pack2(y[6], y[7]);
      const unsigned dst = (ffn ? (unsigned)(OFF_XA + 2 * MB) : (unsigned)(OFF_XF + (size_t)c * 2 * MB)) + (unsigned)((s * SEG + t) * 1024 + lane * 16);
      st16(rs, dst, o);
      LDSWAIT();
      DRAIN();
      const unsigned f = ffn ? (unsigned)(OFF_F_X2 + (size_t)s * FLINE) : (unsigned)(OFF_F_X1 + (size_t)(c * NSEG + s) * FLINE);
      if (lane == 0) st4(rs, f + (unsigned)t * 4u, 1u);
    }
  }
}

__global__ void __launch_bounds__(256, 1) k_main(Params p) {
  __shared__ __attribute__((aligned(16))) unsigned char lds[LDS_BYTES];
  LAS unsigned char* L = (LAS unsigned char*)lds;
  const rsrc_t rs = __builtin_amdgcn_make_buffer_rsrc((void*)p.ws, 0, (int)WS_BYTES, 0x00020000);
  const int b = blockIdx.x;
  if (b < 64) role_chain(p, rs, L, b >> 5, b & 31, b & 7);
  else if (b < 96) role_creduce(rs, (b - 64) >> 4, (b - 64) & 15);
  else if (b < 160) role_qkv(rs, L, (b - 96) >> 5, (b - 96) & 31);
  else if (b < 192) role_att(rs, L, (b - 160) >> 4, (b - 160) & 15);
  else if (b < 220) role_ffn(rs, L, (b - 192) / NFFN, (b - 192) % NFFN);
  else if (b < 252) role_red(p, rs, L, b - 220);
}

extern "C" void kernel_launch(void* const* d_in, const int* in_sizes, int n_in,
                              void* d_out, int out_size, void* d_ws, size_t ws_size,
                              hipStream_t stream) {
  PrepParams pp;
  pp.x = (const u16*)d_in[0];
  pp.wq = (const u16*)d_in[2];
  pp.wk = (const u16*)d_in[3];
  pp.wv = (const u16*)d_in[4];
  pp.wo = (const u16*)d_in[5];
  pp.wg = (const u16*)d_in[8];
  pp.wu = (const u16*)d_in[9];
  pp.wd = (const u16*)d_in[10];
  pp.ws = (unsigned char*)d_ws;
  k_prep<<<NJ_ALL, 256, 0, stream>>>(pp);
  Params p;
  p.x = (const u16*)d_in[0];
  p.mem0 = (const u16*)d_in[1];
  p.out = (u16*)d_out;
  p.ws = (unsigned char*)d_ws;
  k_main<<<252, 256, 0, stream>>>(p);
}
```

```cpp
#include <hip/hip_runtime.h>
#include <hip/hip_bf16.h>
#include <stdint.h>

#define GAS __attribute__((address_space(1)))
#define LAS __attribute__((address_space(3)))
typedef unsigned v4u __attribute__((ext_vector_type(4)));
typedef unsigned v2u __attribute__((ext_vector_type(2)));
typedef float f32x4 __attribute__((ext_vector_type(4)));
typedef float f32x2 __attribute__((ext_vector_type(2)));
typedef short bf16x8 __attribute__((ext_vector_type(8)));
typedef __bf16 bf16x2 __attribute__((ext_vector_type(2)));
typedef GAS unsigned gu32;
typedef unsigned short u16;
#define RLX_AGENT __ATOMIC_RELAXED, __HIP_MEMORY_SCOPE_AGENT
#define SB() __builtin_amdgcn_sched_barrier(0)
#define DRAIN() asm volatile("s_waitcnt vmcnt(0)" ::: "memory")
#define LDSWAIT() asm volatile("s_waitcnt lgkmcnt(0)" ::: "memory")

constexpr int S_TOK = 2048, DM = 512, NH = 32, DH = 128, NMEM = 8, SEG = 32, NSEG = 64, DFF = 1792;
constexpr int NFFN = 14, NATT = 16;

constexpr size_t KB = 1024, MB = 1024 * 1024;
constexpr size_t OFF_ABORT = 0;
enum { K_QKV = 0, K_KM = 1, K_ATT = 2, K_X1 = 3, K_FFN = 4, K_X2 = 5, K_COL = 6 };
constexpr size_t OFF_CNT = 4 * KB;
__host__ __device__ constexpr unsigned cnt_off(int kind, int c, int idx) { return (unsigned)(OFF_CNT + (size_t)((kind * 2 + c) * 32 + idx) * 128); }
constexpr size_t ZERO_BYTES = 64 * KB;
constexpr size_t ROW = 32 * KB;
constexpr size_t OFF_WQKV = 2 * MB;
constexpr size_t OFF_WO = OFF_WQKV + 12 * MB;
constexpr size_t OFF_WG = OFF_WO + 4 * MB;
constexpr size_t OFF_WU = OFF_WG + NFFN * 128 * KB;
constexpr size_t OFF_WD = OFF_WU + NFFN * 128 * KB;
constexpr size_t OFF_XA = 24 * MB;
constexpr size_t OFF_XF = OFF_XA + 4 * MB;
constexpr size_t OFF_KVM = OFF_XF + 4 * MB;
constexpr size_t OFF_QKVF = OFF_KVM + 16 * MB;
constexpr size_t OFF_PO = OFF_QKVF + 96 * MB;
constexpr size_t OFF_PF = OFF_PO + 32 * MB;
constexpr size_t OFF_P = OFF_PF + 28 * MB;
constexpr size_t CR_BLK = 14 * ROW, P_CHAIN = 16 * CR_BLK, M_ROW0 = 8 * ROW, M_SLOT = 2 * ROW;
constexpr size_t WS_BYTES = OFF_P + 2 * P_CHAIN;
static_assert(OFF_CNT + 14 * 32 * 128 <= ZERO_BYTES, "flags");
static_assert(OFF_WD + NFFN * 128 * KB <= OFF_XA, "weights");
static_assert(WS_BYTES <= 256 * MB, "workspace");

constexpr unsigned kSpinCap = 1u << 17;
#define CAL_WAIT 0
#define CAL_N 2
#define CR_SLEEP 24
constexpr int LDS_BYTES = 163840;

#define STAMP(s, i) do {} while (0)
#define STAMP0(s, i) do {} while (0)

struct Params {
  const u16* x;
  const u16* mem0;
  u16* out;
  unsigned char* ws;
};
struct PrepParams {
  const u16* x;
  const u16* wq;
  const u16* wk;
  const u16* wv;
  const u16* wo;
  const u16* wg;
  const u16* wu;
  const u16* wd;
  unsigned char* ws;
};

__device__ __forceinline__ unsigned xcc_id() { return (unsigned)__builtin_amdgcn_s_getreg(20 | (3 << 11)) & 7u; }
__device__ __forceinline__ unsigned pack2(float a, float b) {
  f32x2 v = {a, b};
  bf16x2 r = __builtin_convertvector(v, bf16x2);
  return __builtin_bit_cast(unsigned, r);
}
__device__ __forceinline__ float bf16r(float a) { return __uint_as_float(pack2(a, 0.f) << 16); }
__device__ __forceinline__ float bflo(unsigned u) { return __uint_as_float(u << 16); }
__device__ __forceinline__ float bfhi(unsigned u) { return __uint_as_float(u & 0xffff0000u); }
#define MFMA "v_mfma_f32_16x16x32_bf16 "
template <bool FIRST> __device__ __forceinline__ void mfa(f32x4& c, v4u a, v4u b) {
  if (FIRST) asm(MFMA "%0, %1, %2, 0" : "=&v"(c) : "a"(a), "v"(b));
  else asm(MFMA "%0, %1, %2, %0" : "+v"(c) : "a"(a), "v"(b));
}
template <bool FIRST> __device__ __forceinline__ void mfv(f32x4& c, v4u a, v4u b) {
  if (FIRST) asm(MFMA "%0, %1, %2, 0" : "=&v"(c) : "v"(a), "v"(b));
  else asm(MFMA "%0, %1, %2, %0" : "+v"(c) : "v"(a), "v"(b));
}
#define MFA(first, c, a, b) do { if (first) mfa<true>(c, a, b); else mfa<false>(c, a, b); } while (0)
#define MFV(first, c, a, b) do { if (first) mfv<true>(c, a, b); else mfv<false>(c, a, b); } while (0)
__device__ __forceinline__ void mfma4a(f32x4& c0, f32x4& c1, f32x4& c2, f32x4& c3, v4u a0, v4u a1, v4u a2, v4u a3, v4u b) {
  asm("s_nop 1\n\t"
      "v_mfma_f32_16x16x32_bf16 %0, %4, %8, %0\n\t"
      "v_mfma_f32_16x16x32_bf16 %1, %5, %8, %1\n\t"
      "v_mfma_f32_16x16x32_bf16 %2, %6, %8, %2\n\t"
      "v_mfma_f32_16x16x32_bf16 %3, %7, %8, %3"
      : "+v"(c0), "+v"(c1), "+v"(c2), "+v"(c3)
      : "a"(a0), "a"(a1), "a"(a2), "a"(a3), "v"(b));
}
__device__ __forceinline__ void mfma6a(f32x4& c0, f32x4& c1, f32x4& c2, f32x4& c3, f32x4& c4, f32x4& c5,
                                       v4u a0, v4u a1, v4u a2, v4u a3, v4u b4, v4u b5, v4u b) {
  asm("s_nop 1\n\t"
      MFMA "%0, %6, %12, %0\n\t"
      MFMA "%1, %7, %12, %1\n\t"
      MFMA "%2, %8, %12, %2\n\t"
      MFMA "%3, %9, %12, %3\n\t"
      MFMA "%4, %12, %10, %4\n\t"
      MFMA "%5, %12, %11, %5"
      : "+v"(c0), "+v"(c1), "+v"(c2), "+v"(c3), "+v"(c4), "+v"(c5)
      : "a"(a0), "a"(a1), "a"(a2), "a"(a3), "v"(b4), "v"(b5), "v"(b));
}
__device__ __forceinline__ void mfma2v(f32x4& c0, f32x4& c1, v4u a, v4u b0, v4u b1) {
  asm("s_nop 1\n\t"
      MFMA "%0, %2, %3, %0\n\t"
      MFMA "%1, %2, %4, %1"
      : "+v"(c0), "+v"(c1)
      : "v"(a), "v"(b0), "v"(b1));
}
__device__ __forceinline__ void mfma4v(f32x4& c0, f32x4& c1, f32x4& c2, f32x4& c3, v4u a0, v4u a1, v4u a2, v4u a3, v4u b) {
  asm("s_nop 1\n\t"
      MFMA "%0, %4, %8, %0\n\t"
      MFMA "%1, %5, %8, %1\n\t"
      MFMA "%2, %6, %8, %2\n\t"
      MFMA "%3, %7, %8, %3"
      : "+v"(c0), "+v"(c1), "+v"(c2), "+v"(c3)
      : "v"(a0), "v"(a1), "v"(a2), "v"(a3), "v"(b));
}
__device__ __forceinline__ void mfma3v(f32x4& c0, f32x4& c1, f32x4& c2, v4u a0, v4u a1, v4u a2, v4u b) {
  asm("s_nop 1\n\t"
      MFMA "%0, %3, %6, %0\n\t"
      MFMA "%1, %4, %6, %1\n\t"
      MFMA "%2, %5, %6, %2"
      : "+v"(c0), "+v"(c1), "+v"(c2)
      : "v"(a0), "v"(a1), "v"(a2), "v"(b));
}
__device__ __forceinline__ void mfma_pv(f32x4& c0, f32x4& c1, v4u a0, v4u a1, v4u m0, v4u m1, v4u p0, v4u p1) {
  asm("s_nop 1\n\t"
      MFMA "%0, %2, %6, 0\n\t"
      MFMA "%1, %3, %6, 0\n\t"
      MFMA "%0, %4, %7, %0\n\t"
      MFMA "%1, %5, %7, %1"
      : "=&v"(c0), "=&v"(c1)
      : "v"(a0), "v"(a1), "v"(m0), "v"(m1), "v"(p0), "v"(p1));
}
__device__ __forceinline__ void mfma_k4(f32x4& c, v4u a0, v4u a1, v4u a2, v4u a3, v4u b0, v4u b1, v4u b2, v4u b3) {
  asm("s_nop 1\n\t"
      MFMA "%0, %1, %5, 0\n\t"
      MFMA "%0, %2, %6, %0\n\t"
      MFMA "%0, %3, %7, %0\n\t"
      MFMA "%0, %4, %8, %0"
      : "=&v"(c)
      : "v"(a0), "v"(a1), "v"(a2), "v"(a3), "v"(b0), "v"(b1), "v"(b2), "v"(b3));
}
__device__ __forceinline__ void mfma_settle2(f32x4& c0, f32x4& c1) { asm("s_nop 7\n\ts_nop 1" : "+v"(c0), "+v"(c1)); }
__device__ __forceinline__ void mfma_settle3(f32x4& c0, f32x4& c1, f32x4& c2) { asm("s_nop 7\n\ts_nop 1" : "+v"(c0), "+v"(c1), "+v"(c2)); }
__device__ __forceinline__ void mfma_settle(f32x4& c0, f32x4& c1, f32x4& c2, f32x4& c3) {
  asm("s_nop 7\n\ts_nop 1" : "+v"(c0), "+v"(c1), "+v"(c2), "+v"(c3));
}
__device__ __forceinline__ v4u pack8(f32x4 a, f32x4 b) {
  v4u r;
  r.x = pack2(a.x, a.y); r.y = pack2(a.z, a.w); r.z = pack2(b.x, b.y); r.w = pack2(b.z, b.w);
  return r;
}
__device__ __forceinline__ float xmax(float x) {
  auto s = __builtin_amdgcn_permlane16_swap(__float_as_uint(x), __float_as_uint(x), false, false);
  x = fmaxf(__uint_as_float(s[0]), __uint_as_float(s[1]));
  auto t = __builtin_amdgcn_permlane32_swap(__float_as_uint(x), __float_as_uint(x), false, false);
  return fmaxf(__uint_as_float(t[0]), __uint_as_float(t[1]));
}
__device__ __forceinline__ float xsum(float x) {
  auto s = __builtin_amdgcn_permlane16_swap(__float_as_uint(x), __float_as_uint(x), false, false);
  x = __uint_as_float(s[0]) + __uint_as_float(s[1]);
  auto t = __builtin_amdgcn_permlane32_swap(__float_as_uint(x), __float_as_uint(x), false, false);
  return __uint_as_float(t[0]) + __uint_as_float(t[1]);
}
__device__ __forceinline__ float wave_sum(float x) {
  x += __shfl_xor(x, 1); x += __shfl_xor(x, 2); x += __shfl_xor(x, 4); x += __shfl_xor(x, 8);
  return xsum(x);
}
typedef __amdgpu_buffer_rsrc_t rsrc_t;
__device__ __forceinline__ v4u ld16(rsrc_t rs, unsigned off) { return __builtin_amdgcn_raw_buffer_load_b128(rs, (int)off, 0, 16); }
__device__ __forceinline__ v4u ld16s(rsrc_t rs, unsigned voff, unsigned soff) { return __builtin_amdgcn_raw_buffer_load_b128(rs, (int)voff, (int)soff, 16); }
__device__ __forceinline__ v2u ld8(rsrc_t rs, unsigned off) { return __builtin_amdgcn_raw_buffer_load_b64(rs, (int)off, 0, 16); }
__device__ __forceinline__ v2u ld8s(rsrc_t rs, unsigned voff, unsigned soff) { return __builtin_amdgcn_raw_buffer_load_b64(rs, (int)voff, (int)soff, 16); }
__device__ __forceinline__ unsigned ld4(rsrc_t rs, unsigned off) { return __builtin_amdgcn_raw_buffer_load_b32(rs, (int)off, 0, 16); }
__device__ __forceinline__ v4u ld16p(rsrc_t rs, unsigned off) { return __builtin_amdgcn_raw_buffer_load_b128(rs, (int)off, 0, 0); }
__device__ __forceinline__ void st16(rsrc_t rs, unsigned off, v4u v) { __builtin_amdgcn_raw_buffer_store_b128(v, rs, (int)off, 0, 16); }
__device__ __forceinline__ void st16s(rsrc_t rs, unsigned voff, unsigned soff, v4u v) { __builtin_amdgcn_raw_buffer_store_b128(v, rs, (int)voff, (int)soff, 16); }
__device__ __forceinline__ void st8(rsrc_t rs, unsigned off, v2u v) { __builtin_amdgcn_raw_buffer_store_b64(v, rs, (int)off, 0, 16); }
__device__ __forceinline__ void st4(rsrc_t rs, unsigned off, unsigned v) { __builtin_amdgcn_raw_buffer_store_b32(v, rs, (int)off, 0, 16); }

__device__ __forceinline__ bool spin_fail(unsigned& spins, rsrc_t rs) {
  ++spins;
  if (spins > kSpinCap) { st4(rs, (unsigned)OFF_ABORT, 1u); return true; }
  if ((spins & 127u) == 0u) return __builtin_amdgcn_readfirstlane(ld4(rs, (unsigned)OFF_ABORT)) != 0u;
  return false;
}
template <int SLEEP>
__device__ __forceinline__ void wait_cnt(rsrc_t rs, unsigned off, int n, int probe, unsigned want, int lane) {
  unsigned spins = 0;
  for (;;) {
    const unsigned v = ld4(rs, off + (unsigned)probe * 128u);
    if (__builtin_amdgcn_readfirstlane(v) >= want) break;
    if (spin_fail(spins, rs)) break;
    __builtin_amdgcn_s_sleep(SLEEP);
  }
  if (n > 1) {
    for (;;) {
      const unsigned v = ld4(rs, off + (unsigned)(lane < n ? lane : 0) * 128u);
      if (__all(v >= want)) break;
      if (spin_fail(spins, rs)) break;
      __builtin_amdgcn_s_sleep(2);
    }
  }
  __builtin_amdgcn_fence(__ATOMIC_ACQUIRE, "wavefront");
}

__device__ __forceinline__ unsigned wait_table(rsrc_t rs, int c, int lane) {
  unsigned spins = 0, v;
  for (;;) {
    v = ld4(rs, cnt_off(K_COL, c, 0) + (unsigned)(lane & 15) * 128u);
    if (__all(v != 0u)) break;
    if (spin_fail(spins, rs)) break;
    __builtin_amdgcn_s_sleep(8);
  }
  return v - 1u;
}
__device__ __forceinline__ unsigned mem_copy(unsigned tab, unsigned x) {
  const unsigned long long m = __ballot(((tab >> 3) & 7u) == x) & 0xffffull;
  const int own = m == 0ull ? 0 : __builtin_ctzll(m);
  return (unsigned)own * (unsigned)CR_BLK + (unsigned)M_ROW0 + ((unsigned)__builtin_amdgcn_readlane(tab, own) & 7u) * 4096u;
}

__device__ __forceinline__ void layernorm8(const float (&x)[8], float (&y)[8]) {
  float s = 0.f;
#pragma unroll
  for (int j = 0; j < 8; ++j) s += x[j];
  const float mean = wave_sum(s) * (1.0f / DM);
  float q = 0.f;
#pragma unroll
  for (int j = 0; j < 8; ++j) { const float d = x[j] - mean; q += d * d; }
  const float var = wave_sum(q) * (1.0f / DM);
  const float m = bf16r(mean);
  const float den = bf16r(sqrtf(bf16r(bf16r(var) + 1e-5f)));
#pragma unroll
  for (int j = 0; j < 8; ++j) y[j] = bf16r(x[j] - m) / den;
}

constexpr int NJ_QKV = 3 * 16 * 32;
constexpr int NJ_WO = 32 * 4 * 4;
constexpr int NJ_GU = 2 * 16 * NFFN;
constexpr int NJ_WD = NFFN * 4 * 4;
constexpr int NJ_LN = S_TOK / 4;
constexpr int NJ_INIT = 256;
constexpr int NJ_ALL = NJ_QKV + NJ_WO + NJ_GU + NJ_WD + NJ_LN + NJ_INIT;

__global__ void __launch_bounds__(256) k_prep(PrepParams p) {
  __shared__ __attribute__((aligned(16))) u16 tile[32][136];
  const int tid = threadIdx.x, lane = tid & 63, w = tid >> 6;
  int job = blockIdx.x;
  if (job < NJ_QKV + NJ_WO + NJ_GU + NJ_WD) {
    const u16* src; int ld; bool pi; size_t dst; size_t tstride;
    if (job < NJ_QKV) {
      const int m = job / 512, ks = (job >> 5) & 15, h = job & 31;
      const u16* W = m == 0 ? p.wq : (m == 1 ? p.wk : p.wv);
      src = W + (size_t)(32 * ks) * (NH * DH) + h * DH; ld = NH * DH; pi = false;
      dst = OFF_WQKV + ((size_t)(m * 32 + h) * 8 * 16 + ks) * KB; tstride = 16 * KB;
    } else if (job < NJ_QKV + NJ_WO) {
      job -= NJ_QKV;
      const int h = job >> 4, ks = (job >> 2) & 3, nb = job & 3;
      src = p.wo + (size_t)(h * DH + 32 * ks) * DM + nb * 128; ld = DM; pi = true;
      dst = OFF_WO + ((size_t)(h * 32 + nb * 8) * 4 + ks) * KB; tstride = 4 * KB;
    } else if (job < NJ_QKV + NJ_WO + NJ_GU) {
      job -= NJ_QKV + NJ_WO;
      const int m = job / (16 * NFFN), r = job % (16 * NFFN), ks = r / NFFN, sl = r % NFFN;
      src = (m == 0 ? p.wg : p.wu) + (size_t)(32 * ks) * DFF + sl * 128; ld = DFF; pi = false;
      dst = (m == 0 ? OFF_WG : OFF_WU) + ((size_t)sl * 8 * 16 + ks) * KB; tstride = 16 * KB;
    } else {
      job -= NJ_QKV + NJ_WO + NJ_GU;
      const int sl = job >> 4, ks = (job >> 2) & 3, nb = job & 3;
      src = p.wd + (size_t)(sl * 128 + 32 * ks) * DM + nb * 128; ld = DM; pi = true;
      dst = OFF_WD + ((size_t)(sl * 32 + nb * 8) * 4 + ks) * KB; tstride = 4 * KB;
    }
#pragma unroll
    for (int it = 0; it < 2; ++it) {
      const int idx = it * 256 + tid, r = idx >> 4, c = idx & 15;
      const v4u v = *(const v4u*)(src + (size_t)r * ld + c * 8);
      *(v4u*)&tile[r][c * 8] = v;
    }
    __syncthreads();
    const int g = lane >> 4, r = lane & 15;
#pragma unroll
    for (int it = 0; it < 2; ++it) {
      const int t = w * 2 + it;
      unsigned e[8];
#pragma unroll
      for (int j = 0; j < 8; ++j) {
        const int k = pi ? (16 * (j >> 2) + 4 * g + (j & 3)) : (8 * g + j);
        e[j] = tile[k][16 * t + r];
      }
      v4u o; o.x = e[0] | (e[1] << 16); o.y = e[2] | (e[3] << 16); o.z = e[4] | (e[5] << 16); o.w = e[6] | (e[7] << 16);
      *(v4u*)(p.ws + dst + (size_t)t * tstride + lane * 16) = o;
    }
    return;
  }
  job -= NJ_QKV + NJ_WO + NJ_GU + NJ_WD;
  if (job < NJ_LN) {
    const int row = job * 4 + w;
    const v4u v = *(const v4u*)(p.x + (size_t)row * DM + lane * 8);
    float x[8] = {bflo(v.x), bfhi(v.x), bflo(v.y), bfhi(v.y), bflo(v.z), bfhi(v.z), bflo(v.w), bfhi(v.w)}, y[8];
    layernorm8(x, y);
    v4u o; o.x = pack2(y[0], y[1]); o.y = pack2(y[2], y[3]); o.z = pack2(y[4], y[5]); o.w = pack2(y[6], y[7]);
    *(v4u*)(p.ws + OFF_XA + (size_t)row * 1024 + lane * 16) = o;
    return;
  }
  job -= NJ_LN;
  {
    const v4u z = {0u, 0u, 0u, 0u};
    for (size_t i = (size_t)job * 256 + tid; i < ZERO_BYTES / 16; i += (size_t)NJ_INIT * 256) *(v4u*)(p.ws + i * 16) = z;
    *(v4u*)(p.ws + OFF_P + (size_t)(job >> 3) * CR_BLK + (job & 7) * 4096 + tid * 16) = z;
  }
}

constexpr float kScale = 0.08838834764831845f;
constexpr float kNegBig = -3.0e38f;
constexpr float kLog2e = 1.4426950408889634f;

__device__ __forceinline__ void softmax_frags(f32x4 sx0, f32x4 sx1, f32x4 sm, unsigned ok0, unsigned ok1, bool okm, v4u& p0, v4u& p1) {
  unsigned u[6] = {pack2(sx0.x, sx0.y), pack2(sx0.z, sx0.w), pack2(sx1.x, sx1.y), pack2(sx1.z, sx1.w), pack2(sm.x, sm.y), pack2(sm.z, sm.w)};
  float a[12];
#pragma unroll
  for (int j = 0; j < 6; ++j) {
    u[j] = pack2(bflo(u[j]) * kScale, bfhi(u[j]) * kScale);
    a[2 * j] = bflo(u[j]); a[2 * j + 1] = bfhi(u[j]);
  }
#pragma unroll
  for (int r = 0; r < 4; ++r) {
    a[r] = ((ok0 >> r) & 1u) ? a[r] : kNegBig;
    a[4 + r] = ((ok1 >> r) & 1u) ? a[4 + r] : kNegBig;
    a[8 + r] = okm ? a[8 + r] : kNegBig;
  }
  float mx = fmaxf(fmaxf(fmaxf(a[0], a[1]), fmaxf(a[2], a[3])), fmaxf(fmaxf(a[4], a[5]), fmaxf(a[6], a[7])));
  mx = fmaxf(mx, fmaxf(fmaxf(a[8], a[9]), fmaxf(a[10], a[11])));
  mx = xmax(mx);
  const float nm = -mx * kLog2e;
#pragma unroll
  for (int j = 0; j < 12; ++j) a[j] = __builtin_amdgcn_exp2f(fmaf(a[j], kLog2e, nm));
  float sum = ((a[0] + a[1]) + (a[2] + a[3])) + ((a[4] + a[5]) + (a[6] + a[7])) + ((a[8] + a[9]) + (a[10] + a[11]));
  sum = xsum(sum);
  const float inv = __builtin_amdgcn_rcpf(sum);
  p0.x = pack2(a[0] * inv, a[1] * inv); p0.y = pack2(a[2] * inv, a[3] * inv);
  p0.z = pack2(a[4] * inv, a[5] * inv); p0.w = pack2(a[6] * inv, a[7] * inv);
  p1.x = pack2(a[8] * inv, a[9] * inv); p1.y = pack2(a[10] * inv, a[11] * inv);
  p1.z = 0u; p1.w = 0u;
}

__device__ __forceinline__ void rows_load(rsrc_t rs, unsigned goff, v4u (&v)[8], int w, int lane) {
#pragma unroll
  for (int i = 0; i < 8; ++i) v[i] = ld16s(rs, (unsigned)lane * 16u + (unsigned)(i & 3) * 1024u, goff + (unsigned)(8 * w + (i & 4)) * 1024u);
  SB();
}
constexpr int RSTR = 1040;
constexpr int IMG = 32 * RSTR;
__device__ __forceinline__ void rows_put(LAS unsigned char* L, const v4u (&v)[8], int w, int lane) {
#pragma unroll
  for (int i = 0; i < 8; ++i) *(LAS v4u*)(L + (8 * w + i) * RSTR + lane * 16) = v[i];
}
__device__ __forceinline__ v4u row_frag(const LAS unsigned char* L, int tt, int ks, int fb) {
  return *(const LAS v4u*)(L + fb + ks * 64 + tt * 16 * RSTR);
}
__device__ __forceinline__ void partial_put(LAS unsigned char* L, const f32x4 (&acc)[8][2], int w, int lane) {
  const int g = lane >> 4, i = lane & 15;
  LAS unsigned char* B = L + i * RSTR + w * 256 + g * 8;
#pragma unroll
  for (int dt = 0; dt < 8; ++dt)
#pragma unroll
    for (int tt = 0; tt < 2; ++tt) {
      v2u o; o.x = pack2(acc[dt][tt].x, acc[dt][tt].y); o.y = pack2(acc[dt][tt].z, acc[dt][tt].w);
      *(LAS v2u*)(B + tt * 16 * RSTR + dt * 32) = o;
    }
}
__device__ __forceinline__ void partial_store(rsrc_t rs, unsigned goff, const LAS unsigned char* L, int w, int lane) {
#pragma unroll
  for (int hf = 0; hf < 2; ++hf) {
    v4u v[4];
#pragma unroll
    for (int k = 0; k < 4; ++k) v[k] = *(const LAS v4u*)(L + (8 * w + 4 * hf + k) * RSTR + lane * 16);
#pragma unroll
    for (int k = 0; k < 4; ++k) st16s(rs, (unsigned)lane * 16u + (unsigned)k * 1024u, goff + (unsigned)(8 * w + 4 * hf) * 1024u, v[k]);
    SB();
  }
}

constexpr int CH_KV = 0, CH_MEM = 16384, CH_EX = CH_MEM + 8192, CH_WV = CH_EX + 6144;
__device__ __forceinline__ void role_chain(const Params& p, rsrc_t rs, LAS unsigned char* L, int c, int h) {
  const int tid = threadIdx.x, lane = tid & 63, w = __builtin_amdgcn_readfirstlane(tid >> 6);
  const int g = lane >> 4, r = lane & 15, r8 = lane & 7;
  v4u wq[2][16], wk[2][16], wo[8][4];
#pragma unroll
  for (int t = 0; t < 2; ++t)
#pragma unroll
    for (int ks = 0; ks < 16; ++ks) {
      const unsigned o = (unsigned)(((h * 8 + 2 * w + t) * 16 + ks) * 1024 + lane * 16);
      wq[t][ks] = ld16p(rs, (unsigned)OFF_WQKV + o);
      wk[t][ks] = ld16p(rs, (unsigned)(OFF_WQKV + 4 * MB) + o);
    }
  for (int i = 0; i < 32; ++i) {
    const unsigned o = (unsigned)((i * 4 + w) * 1024 + lane * 16);
    *(LAS v4u*)(L + CH_WV + o) = ld16p(rs, (unsigned)(OFF_WQKV + 8 * MB + (size_t)h * 128 * KB) + o);
  }
#pragma unroll
  for (int dt = 0; dt < 8; ++dt)
#pragma unroll
    for (int ks = 0; ks < 4; ++ks) wo[dt][ks] = ld16p(rs, (unsigned)OFF_WO + (unsigned)(((h * 32 + 8 * w + dt) * 4 + ks) * 1024 + lane * 16));
  {
#pragma unroll
    for (int it = 0; it < 2; ++it) {
      const int idx = it * 256 + tid, i = idx >> 6, ch = idx & 63;
      const v4u v = *(const v4u*)(p.mem0 + i * DM + ch * 8);
      *(LAS v4u*)(L + CH_MEM + (ch >> 2) * 512 + (ch & 3) * 128 + i * 16) = v;
    }
  }
  const unsigned qkv_base = (unsigned)(OFF_QKVF + (size_t)c * NSEG * NH * 24 * KB) + (unsigned)h * 24u * 1024u + 8192u;
  const unsigned kvm_base = (unsigned)(OFF_KVM + (size_t)c * NSEG * NH * 4 * KB) + (unsigned)h * 4096u;
  const unsigned p_lane = (unsigned)((2 * (r >> 3) + (g >> 1)) * 2 * ROW + (g & 1) * 128 + r8 * 16);
  {
    wait_cnt<8>(rs, cnt_off(K_QKV, c, h), 1, 0, 1u, lane);
    v4u v[4];
#pragma unroll
    for (int i = 0; i < 4; ++i) v[i] = ld16(rs, qkv_base + (unsigned)((4 * w + i) * 1024 + lane * 16));
    SB();
#pragma unroll
    for (int i = 0; i < 4; ++i) *(LAS v4u*)(L + CH_KV + (4 * w + i) * 1024 + lane * 16) = v[i];
  }
  unsigned p_soff[4], m_base;
  {
    const unsigned tab = wait_table(rs, c, lane);
#pragma unroll
    for (int dp = 0; dp < 4; ++dp)
      p_soff[dp] = (unsigned)(OFF_P + (size_t)c * P_CHAIN) + (unsigned)((4 * w + dp) * CR_BLK + (h >> 4) * ROW + (h & 15) * 256) +
                   ((unsigned)__builtin_amdgcn_readlane(tab, 4 * w + dp) & 7u) * 4096u;
    m_base = (unsigned)(OFF_P + (size_t)c * P_CHAIN) + mem_copy(tab, xcc_id()) + (unsigned)((w >> 1) * ROW + (w & 1) * 2048 + lane * 16);
  }
  __syncthreads();
  for (int s = 0; s < NSEG; ++s) {
    STAMP0(s, 0);
    const LAS unsigned char* MF = L + CH_MEM + g * 128 + r8 * 16;
    f32x4 aq0, aq1, ak0, ak1, av0, av1;
    {
      v4u mf[4];
#pragma unroll
      for (int i = 0; i < 4; ++i) mf[i] = *(const LAS v4u*)(MF + i * 512);
      SB();
#pragma unroll
      for (int ks = 0; ks < 16; ++ks) {
        const v4u m = mf[ks & 3];
        MFA(ks == 0, aq0, wq[0][ks], m);
        MFA(ks == 0, aq1, wq[1][ks], m);
        SB();
        if (ks >= 1 && ks + 3 < 16) mf[(ks + 3) & 3] = *(const LAS v4u*)(MF + (ks + 3) * 512);
        MFA(ks == 0, ak0, wk[0][ks], m);
        MFA(ks == 0, ak1, wk[1][ks], m);
        SB();
      }
    }
    const LAS unsigned char* WV = L + CH_WV + 2 * w * 16384 + lane * 16;
    v4u mv[2], w0[2], w1[2];
#pragma unroll
    for (int i = 0; i < 2; ++i) {
      mv[i] = *(const LAS v4u*)(MF + i * 512);
      w0[i] = *(const LAS v4u*)(WV + i * 1024);
      w1[i] = *(const LAS v4u*)(WV + 16384 + i * 1024);
    }
    SB();
#define VSTEP(ks) do { MFV((ks) == 0, av0, mv[(ks) & 1], w0[(ks) & 1]); MFV((ks) == 0, av1, mv[(ks) & 1], w1[(ks) & 1]); \
      if ((ks) + 2 < 16) { mv[(ks) & 1] = *(const LAS v4u*)(MF + ((ks) + 2) * 512); w0[(ks) & 1] = *(const LAS v4u*)(WV + ((ks) + 2) * 1024); \
                           w1[(ks) & 1] = *(const LAS v4u*)(WV + 16384 + ((ks) + 2) * 1024); } SB(); } while (0)
    mfma_settle(aq0, aq1, ak0, ak1);
    if (r < 8) {
      *(LAS v4u*)(L + CH_EX + w * 512 + g * 128 + r * 16) = pack8(ak0, ak1);
      *(LAS v4u*)(L + CH_EX + 2048 + w * 512 + g * 128 + r * 16) = pack8(aq0, aq1);
    }
    SB();
    v4u kx[4];
#pragma unroll
    for (int i = 0; i < 2; ++i) {
      kx[i] = *(const LAS v4u*)(L + CH_KV + i * 1024 + lane * 16);
      kx[2 + i] = *(const LAS v4u*)(L + CH_KV + (4 + i) * 1024 + lane * 16);
    }
    SB();
    VSTEP(0);
    __syncthreads();
    STAMP0(s, 1);
    const LAS unsigned char* EXL = L + CH_EX + g * 128 + r8 * 16;
    v4u qf[2], km[2];
#pragma unroll
    for (int i = 0; i < 2; ++i) {
      qf[i] = *(const LAS v4u*)(EXL + 2048 + i * 512);
      km[i] = *(const LAS v4u*)(EXL + i * 512);
    }
    SB();
    VSTEP(1);
    f32x4 sx0, sx1, sm;
#pragma unroll
    for (int i = 0; i < 2; ++i) {
      MFV(i == 0, sx0, kx[i], qf[i]); MFV(i == 0, sx1, kx[2 + i], qf[i]); MFV(i == 0, sm, km[i], qf[i]);
      SB();
      qf[i] = *(const LAS v4u*)(EXL + 2048 + (2 + i) * 512);
      km[i] = *(const LAS v4u*)(EXL + (2 + i) * 512);
      kx[i] = *(const LAS v4u*)(L + CH_KV + (2 + i) * 1024 + lane * 16);
      kx[2 + i] = *(const LAS v4u*)(L + CH_KV + (6 + i) * 1024 + lane * 16);
      SB();
    }
    VSTEP(2);
    VSTEP(3);
#pragma unroll
    for (int i = 0; i < 2; ++i) { MFV(false, sx0, kx[i], qf[i]); MFV(false, sx1, kx[2 + i], qf[i]); MFV(false, sm, km[i], qf[i]); }
    v4u vx[2];
#pragma unroll
    for (int t = 0; t < 2; ++t) vx[t] = *(const LAS v4u*)(L + CH_KV + 8192 + (2 * w + t) * 1024 + lane * 16);
    SB();
    VSTEP(4);
    mfma_settle3(sx0, sx1, sm);
    float a[12];
    {
      unsigned u0 = pack2(sx0.x, sx0.y), u1 = pack2(sx0.z, sx0.w), u2 = pack2(sx1.x, sx1.y), u3 = pack2(sx1.z, sx1.w);
      u0 = pack2(bflo(u0) * kScale, bfhi(u0) * kScale); u1 = pack2(bflo(u1) * kScale, bfhi(u1) * kScale);
      SB();
      VSTEP(5);
      u2 = pack2(bflo(u2) * kScale, bfhi(u2) * kScale); u3 = pack2(bflo(u3) * kScale, bfhi(u3) * kScale);
      a[0] = bflo(u0); a[1] = bfhi(u0); a[2] = bflo(u1); a[3] = bfhi(u1);
      SB();
      VSTEP(6);
      unsigned u4 = pack2(sm.x, sm.y), u5 = pack2(sm.z, sm.w);
      u4 = pack2(bflo(u4) * kScale, bfhi(u4) * kScale); u5 = pack2(bflo(u5) * kScale, bfhi(u5) * kScale);
      a[4] = bflo(u2); a[5] = bfhi(u2); a[6] = bflo(u3); a[7] = bfhi(u3);
      SB();
      VSTEP(7);
      a[8] = g < 2 ? bflo(u4) : kNegBig; a[9] = g < 2 ? bfhi(u4) : kNegBig;
      a[10] = g < 2 ? bflo(u5) : kNegBig; a[11] = g < 2 ? bfhi(u5) : kNegBig;
    }
    float mx = fmaxf(fmaxf(fmaxf(a[0], a[1]), fmaxf(a[2], a[3])), fmaxf(fmaxf(a[4], a[5]), fmaxf(a[6], a[7])));
    mx = fmaxf(mx, fmaxf(fmaxf(a[8], a[9]), fmaxf(a[10], a[11])));
    SB();
    VSTEP(8);
    mx = xmax(mx);
    const float nm = -mx * kLog2e;
    SB();
    VSTEP(9);
#pragma unroll
    for (int j = 0; j < 4; ++j) a[j] = __builtin_amdgcn_exp2f(fmaf(a[j], kLog2e, nm));
    SB();
    VSTEP(10);
#pragma unroll
    for (int j = 4; j < 8; ++j) a[j] = __builtin_amdgcn_exp2f(fmaf(a[j], kLog2e, nm));
    SB();
    VSTEP(11);
#pragma unroll
    for (int j = 8; j < 12; ++j) a[j] = __builtin_amdgcn_exp2f(fmaf(a[j], kLog2e, nm));
    SB();
    VSTEP(12);
    float sum = ((a[0] + a[1]) + (a[2] + a[3])) + ((a[4] + a[5]) + (a[6] + a[7])) + ((a[8] + a[9]) + (a[10] + a[11]));
    SB();
    VSTEP(13);
    sum = xsum(sum);
    const float inv = __builtin_amdgcn_rcpf(sum);
    SB();
    VSTEP(14);
    VSTEP(15);
#undef VSTEP
    v4u p0, p1;
    p0.x = pack2(a[0] * inv, a[1] * inv); p0.y = pack2(a[2] * inv, a[3] * inv);
    p0.z = pack2(a[4] * inv, a[5] * inv); p0.w = pack2(a[6] * inv, a[7] * inv);
    p1.x = pack2(a[8] * inv, a[9] * inv); p1.y = pack2(a[10] * inv, a[11] * inv); p1.z = 0u; p1.w = 0u;
    STAMP0(s, 6);
    mfma_settle2(av0, av1);
    v4u vm0, vm1;
    vm0.x = pack2(av0.x, av0.y); vm0.y = pack2(av0.z, av0.w); vm0.z = 0u; vm0.w = 0u;
    vm1.x = pack2(av1.x, av1.y); vm1.y = pack2(av1.z, av1.w); vm1.z = 0u; vm1.w = 0u;
    {
      f32x4 o0, o1;
      mfma_pv(o0, o1, vx[0], vx[1], vm0, vm1, p0, p1);
      mfma_settle2(o0, o1);
      if (r < 8) *(LAS v4u*)(L + CH_EX + 4096 + w * 512 + g * 128 + r * 16) = pack8(o0, o1);
    }
    __syncthreads();
    STAMP0(s, 7);
    const unsigned tag = (unsigned)(s + 1) & 3u;
    if (s + 1 < NSEG) {
      v4u of[4];
#pragma unroll
      for (int ks = 0; ks < 4; ++ks) of[ks] = *(const LAS v4u*)(EXL + 4096 + ks * 512);
      SB();
      f32x4 pa[8];
#pragma unroll
      for (int dp = 0; dp < 5; ++dp) {
        if (dp < 4) {
#pragma unroll
          for (int ks = 0; ks < 4; ++ks) {
            MFV(ks == 0, pa[2 * dp], wo[2 * dp][ks], of[ks]);
            MFV(ks == 0, pa[2 * dp + 1], wo[2 * dp + 1][ks], of[ks]);
          }
        }
        if (dp == 4) mfma_settle2(pa[6], pa[7]);
        SB();
        if (dp >= 1) {
          const f32x4 a4 = r < 8 ? pa[2 * dp - 2] : pa[2 * dp - 1];
          v4u o;
          o.x = (__float_as_uint(a4.x) & ~3u) | tag; o.y = (__float_as_uint(a4.y) & ~3u) | tag;
          o.z = (__float_as_uint(a4.z) & ~3u) | tag; o.w = (__float_as_uint(a4.w) & ~3u) | tag;
          st16s(rs, p_lane, p_soff[dp - 1], o);
        }
        SB();
      }
    }
    STAMP0(s, 2);
    {
      const unsigned kb = kvm_base + (unsigned)(s * NH * 4096);
      if (w < 2) st16(rs, kb + (unsigned)(w * 1024 + lane * 16), *(const LAS v4u*)(L + CH_EX + w * 1024 + lane * 16));
      if (g < 2) {
        st8(rs, kb + (unsigned)(2048 + (2 * w) * 256 + g * 128 + r * 8), v2u{vm0.x, vm0.y});
        st8(rs, kb + (unsigned)(2048 + (2 * w + 1) * 256 + g * 128 + r * 8), v2u{vm1.x, vm1.y});
      }
      DRAIN();
    }
    __syncthreads();
    if (tid == 0) st4(rs, cnt_off(K_KM, c, h), (unsigned)(s + 1));
    if (s + 1 < NSEG) {
      STAMP0(s, 3);
      wait_cnt<4>(rs, cnt_off(K_QKV, c, h), 1, 0, (unsigned)(s + 2), lane);
      v4u v[4];
#pragma unroll
      for (int i = 0; i < 4; ++i) v[i] = ld16(rs, qkv_base + (unsigned)((s + 1) * NH * 24 * 1024 + (4 * w + i) * 1024 + lane * 16));
      SB();
#pragma unroll
      for (int i = 0; i < 4; ++i) *(LAS v4u*)(L + CH_KV + (4 * w + i) * 1024 + lane * 16) = v[i];
      STAMP0(s, 4);
      const unsigned mo = m_base + (unsigned)(((s + 1) % 3) * M_SLOT);
      v4u x0, x1; unsigned spins = 0;
      for (;;) {
        x0 = ld16(rs, mo); x1 = ld16(rs, mo + 1024u);
        SB();
        const unsigned mx = max(max(max(x0.x, x0.y), max(x0.z, x0.w)), max(max(x1.x, x1.y), max(x1.z, x1.w)));
        if (__all(mx != 0xffffffffu)) break;
        if (spin_fail(spins, rs)) break;
      }
      STAMP0(s, 5);
      *(LAS v4u*)(L + CH_MEM + w * 2048 + lane * 16) = x0;
      *(LAS v4u*)(L + CH_MEM + w * 2048 + 1024 + lane * 16) = x1;
      __syncthreads();
    }
  }
}

__device__ __forceinline__ void role_creduce(const Params& p, rsrc_t rs, LAS unsigned char* L, int c, int idx) {
  const int tid = threadIdx.x, lane = tid & 63, w = __builtin_amdgcn_readfirstlane(tid >> 6);
  const int v = idx * 4 + w;
  const unsigned l16 = (unsigned)lane * 16u;
  const unsigned xcc = xcc_id();
  const unsigned blk = (unsigned)(OFF_P + (size_t)c * P_CHAIN) + (unsigned)(idx * CR_BLK);
  unsigned col;
  {
    volatile LAS unsigned* T = (volatile LAS unsigned*)L;
    if (w == 0) {
      for (int i = 0; i < CAL_WAIT; ++i) __builtin_amdgcn_s_sleep(127);
      unsigned best = 0xffffffffu, arg = 0;
#pragma unroll 1
      for (int q = 0; q < 8 * CAL_N; ++q) {
        const unsigned t0 = (unsigned)__builtin_amdgcn_s_memtime();
        unsigned y = ld4(rs, blk + (unsigned)((q & 7) * 4096 + (q >> 3) * 1024));
        asm volatile("s_waitcnt vmcnt(0)" : "+v"(y) :: "memory");
        const unsigned t1 = (unsigned)__builtin_amdgcn_s_memtime();
        if (t1 - t0 < best) { best = t1 - t0; arg = (unsigned)q & 7u; }
      }
      if (lane == 0) T[0] = arg;
    }
    __syncthreads();
    col = __builtin_amdgcn_readfirstlane(T[0]);
    const v4u z = {0u, 0u, 0u, 0u}, o = {~0u, ~0u, ~0u, ~0u};
#pragma unroll
    for (int k = 0; k < 8; ++k) st16s(rs, l16 + (unsigned)(k & 3) * 1024u, blk + (unsigned)((2 * w + (k >> 2)) * ROW) + col * 4096u, z);
#pragma unroll
    for (int k = 0; k < 6; ++k) st16s(rs, l16 + (unsigned)w * 1024u, blk + (unsigned)(M_ROW0 + k * ROW) + col * 4096u, o);
    DRAIN();
    __syncthreads();
    if (tid == 0) st4(rs, cnt_off(K_COL, c, idx), 1u + col + 8u * xcc);
  }
  const unsigned p_base = blk + (unsigned)(2 * w * ROW) + col * 4096u;
  unsigned m_off0, m_off1;
  {
    const unsigned tab = wait_table(rs, c, lane);
    const unsigned x0 = (unsigned)lane >> 4;
    unsigned c0 = 0, c1 = 0;
#pragma unroll
    for (unsigned x = 0; x < 4; ++x) {
      const unsigned a = mem_copy(tab, x), b = mem_copy(tab, x + 4u);
      c0 = x0 == x ? a : c0; c1 = x0 == x ? b : c1;
    }
    const unsigned m_in = (unsigned)(OFF_P + (size_t)c * P_CHAIN) + (unsigned)((v >> 5) * ROW + ((v >> 2) & 7) * 512 + (v & 3) * 128 + (lane & 7) * 16 + ((lane >> 3) & 1) * 8);
    m_off0 = m_in + c0;
    m_off1 = m_in + c1;
  }
  for (int s = 0; s + 1 < NSEG; ++s) {
    const unsigned tag = (unsigned)(s + 1) & 3u;
    v4u x[8]; unsigned spins = 0;
    if (s > 0) __builtin_amdgcn_s_sleep(CR_SLEEP);
    for (;;) {
      const v4u y = ld16s(rs, l16 + (unsigned)((w & 1) * 2048), p_base + (unsigned)((w >> 1) * ROW));
      if (__all((((y.x ^ tag) | (y.w ^ tag)) & 3u) == 0u)) break;
      if (spin_fail(spins, rs)) break;
    }
    for (;;) {
#pragma unroll
      for (int k = 0; k < 8; ++k) x[k] = ld16s(rs, l16 + (unsigned)(k & 3) * 1024u, p_base + (unsigned)((k >> 2) * ROW));
      SB();
      unsigned bad = 0;
#pragma unroll
      for (int k = 0; k < 8; ++k) bad |= (x[k].x ^ tag) | (x[k].y ^ tag) | (x[k].z ^ tag) | (x[k].w ^ tag);
      if (__all((bad & 3u) == 0u)) break;
      if (spin_fail(spins, rs)) break;
    }
    STAMP(s, w);
    float a0 = 0.f, a1 = 0.f, a2 = 0.f, a3 = 0.f;
#pragma unroll
    for (int k = 0; k < 8; ++k) {
      a0 += __uint_as_float(x[k].x & ~3u); a1 += __uint_as_float(x[k].y & ~3u);
      a2 += __uint_as_float(x[k].z & ~3u); a3 += __uint_as_float(x[k].w & ~3u);
    }
    a0 = xsum(a0); a1 = xsum(a1); a2 = xsum(a2); a3 = xsum(a3);
    v2u o; o.x = pack2(a0, a1); o.y = pack2(a2, a3);
    if (o.x == 0xffffffffu) o.x = 0xffc0ffc0u;
    if (o.y == 0xffffffffu) o.y = 0xffc0ffc0u;
    const unsigned slot = (unsigned)(((s + 1) % 3) * M_SLOT);
    st8(rs, m_off0 + slot, o);
    st8(rs, m_off1 + slot, o);
    __builtin_amdgcn_s_sleep(16);
    const unsigned old = (unsigned)((s % 3) * M_SLOT);
    const v2u ones = {~0u, ~0u};
    st8(rs, m_off0 + old, ones);
    st8(rs, m_off1 + old, ones);
  }
}

__device__ __forceinline__ void role_qkv(rsrc_t rs, LAS unsigned char* L, int c, int h) {
  const int tid = threadIdx.x, lane = tid & 63, w = __builtin_amdgcn_readfirstlane(tid >> 6);
  const int g = lane >> 4, r = lane & 15;
  v4u wq[2][16], wk[2][16], wv[2][16];
#pragma unroll
  for (int t = 0; t < 2; ++t)
#pragma unroll
    for (int ks = 0; ks < 16; ++ks) {
      const unsigned o = (unsigned)(((h * 8 + 2 * w + t) * 16 + ks) * 1024 + lane * 16);
      wq[t][ks] = ld16p(rs, (unsigned)OFF_WQKV + o);
      wk[t][ks] = ld16p(rs, (unsigned)(OFF_WQKV + 4 * MB) + o);
      wv[t][ks] = ld16p(rs, (unsigned)(OFF_WQKV + 8 * MB) + o);
    }
  const int fb = r * RSTR + g * 16;
  volatile LAS unsigned* RDY = (volatile LAS unsigned*)(L + 2 * IMG);
  const unsigned xbase = (unsigned)(OFF_XA + (size_t)c * 2 * MB);
  bool staged = false;
  for (int s = 0; s < NSEG; ++s) {
    LAS unsigned char* X = L + (s & 1) * IMG;
    if (!staged) {
      if (c == 1) {
        if (w == 0) wait_cnt<8>(rs, cnt_off(K_X2, 0, 0), 32, h, (unsigned)(s + 1), lane);
        __syncthreads();
      }
      STAMP0(s, 0);
      v4u v[8];
      rows_load(rs, xbase + (unsigned)s * 32768u, v, w, lane);
      rows_put(X, v, w, lane);
      __syncthreads();
    }
    STAMP0(s, 1);
    unsigned nx = 0xffffffffu;
    if (c == 1 && w == 0) nx = ld4(rs, cnt_off(K_X2, 0, 0) + (unsigned)(lane & 31) * 128u);
    v4u nv[8];
    if (c == 0 && s + 1 < NSEG) rows_load(rs, xbase + (unsigned)(s + 1) * 32768u, nv, w, lane);
    f32x4 aq[2][2], ak[2][2], av[2][2];
    {
      v4u xf[4];
#pragma unroll
      for (int i = 0; i < 4; ++i) xf[i] = row_frag(X, i & 1, i >> 1, fb);
      SB();
#pragma unroll
      for (int i = 0; i < 32; ++i) {
        const int ks = i >> 1, tt = i & 1;
        const v4u x = xf[i & 3];
        MFA(ks == 0, aq[0][tt], wq[0][ks], x);
        MFA(ks == 0, aq[1][tt], wq[1][ks], x);
        MFA(ks == 0, ak[0][tt], wk[0][ks], x);
        SB();
        if (i >= 1 && i + 3 < 32) xf[(i + 3) & 3] = row_frag(X, (i + 3) & 1, (i + 3) >> 1, fb);
        MFA(ks == 0, ak[1][tt], wk[1][ks], x);
        MFV(ks == 0, av[0][tt], x, wv[0][ks]);
        MFV(ks == 0, av[1][tt], x, wv[1][ks]);
        SB();
      }
    }
    mfma_settle(av[0][0], av[1][0], av[0][1], av[1][1]);
    mfma_settle(aq[0][0], aq[1][0], ak[0][0], ak[1][0]);
    mfma_settle(aq[0][1], aq[1][1], ak[0][1], ak[1][1]);
    STAMP0(s, 2);
    const unsigned ob = (unsigned)(OFF_QKVF + (size_t)((c * NSEG + s) * NH + h) * 24 * KB) + (unsigned)lane * 16u;
#pragma unroll
    for (int tt = 0; tt < 2; ++tt) {
      st16(rs, ob + (unsigned)(8192 + (tt * 4 + w) * 1024), pack8(ak[0][tt], ak[1][tt]));
      st16(rs, ob + (unsigned)((tt * 4 + w) * 1024), pack8(aq[0][tt], aq[1][tt]));
    }
#pragma unroll
    for (int t = 0; t < 2; ++t) st16(rs, ob + (unsigned)(16384 + (2 * w + t) * 1024), pack8(av[t][0], av[t][1]));
    if (c == 0) {
      if (s + 1 < NSEG) rows_put(L + ((s + 1) & 1) * IMG, nv, w, lane);
      staged = true;
    } else if (w == 0) {
      const bool ok = __all(nx >= (unsigned)(s + 2)) && s + 1 < NSEG;
      if (lane == 0) RDY[0] = ok ? 1u : 0u;
    }
    DRAIN();
    __syncthreads();
    if (tid == 0) st4(rs, cnt_off(K_QKV, c, h), (unsigned)(s + 1));
    STAMP0(s, 3);
    if (c == 1) {
      staged = RDY[0] != 0u;
      if (staged) {
        v4u v[8];
        rows_load(rs, xbase + (unsigned)(s + 1) * 32768u, v, w, lane);
        rows_put(L + ((s + 1) & 1) * IMG, v, w, lane);
        __syncthreads();
      }
    }
  }
}

__device__ __forceinline__ void role_att(rsrc_t rs, LAS unsigned char* L, int c, int pp) {
  const int tid = threadIdx.x, lane = tid & 63, w = __builtin_amdgcn_readfirstlane(tid >> 6);
  const int g = lane >> 4, r = lane & 15, r8 = lane & 7;
  v4u wo[2][8][4];
#pragma unroll
  for (int hh = 0; hh < 2; ++hh)
#pragma unroll
    for (int dt = 0; dt < 8; ++dt)
#pragma unroll
      for (int ks = 0; ks < 4; ++ks)
        wo[hh][dt][ks] = ld16p(rs, (unsigned)OFF_WO + (unsigned)((((2 * pp + hh) * 32 + 8 * w + dt) * 4 + ks) * 1024 + lane * 16));
  const int hh = w >> 1, tt = w & 1, h = 2 * pp + hh;
  const int q = 16 * tt + r;
  unsigned ok0 = 0, ok1 = 0;
#pragma unroll
  for (int j = 0; j < 4; ++j) { ok0 |= (4 * g + j <= q ? 1u : 0u) << j; ok1 |= (16 + 4 * g + j <= q ? 1u : 0u) << j; }
  LAS unsigned char* EX = L;
  LAS unsigned char* TR = L + 16384;
  const unsigned qbase = (unsigned)(OFF_QKVF + (size_t)(c * NSEG * NH + h) * 24 * KB);
  const unsigned mbase = (unsigned)(OFF_KVM + (size_t)(c * NSEG * NH + h) * 4 * KB);
  const unsigned l16 = (unsigned)lane * 16u;
  unsigned have_qkv = 0, have_km = 0;
  v4u qf[4], kf[8], vf[8];
  bool loaded = false;
  for (int s = 0; s < NSEG; ++s) {
    if (!loaded) {
      if (have_qkv < (unsigned)(s + 1)) wait_cnt<8>(rs, cnt_off(K_QKV, c, h), 1, 0, (unsigned)(s + 1), lane);
      const unsigned ib = qbase + (unsigned)(s * NH * 24 * 1024);
#pragma unroll
      for (int ks = 0; ks < 4; ++ks) qf[ks] = ld16s(rs, l16 + (unsigned)(ks * 1024), ib + (unsigned)(tt * 4096));
#pragma unroll
      for (int i = 0; i < 8; ++i) kf[i] = ld16s(rs, l16 + (unsigned)((i & 3) * 1024), ib + (unsigned)(8192 + (i & 4) * 1024));
#pragma unroll
      for (int i = 0; i < 8; ++i) vf[i] = ld16s(rs, l16 + (unsigned)((i & 3) * 1024), ib + (unsigned)(16384 + (i & 4) * 1024));
      SB();
    }
    f32x4 sx0, sx1, sm;
#pragma unroll
    for (int ks = 0; ks < 4; ++ks) { MFV(ks == 0, sx0, kf[ks], qf[ks]); MFV(ks == 0, sx1, kf[4 + ks], qf[ks]); }
    SB();
    if (have_km < (unsigned)(s + 1)) wait_cnt<4>(rs, cnt_off(K_KM, c, h), 1, 0, (unsigned)(s + 1), lane);
    STAMP0(s, 0);
    const unsigned mb = mbase + (unsigned)(s * NH * 4096);
    v4u km[4]; v2u vm2[8];
#pragma unroll
    for (int ks = 0; ks < 4; ++ks) km[ks] = ld16s(rs, (unsigned)(ks * 512 + g * 128 + r8 * 16), mb);
#pragma unroll
    for (int et = 0; et < 8; ++et) vm2[et] = ld8s(rs, (unsigned)(2048 + et * 256 + (g & 1) * 128 + r * 8), mb);
    const unsigned nq = ld4(rs, cnt_off(K_QKV, c, h));
    SB();
#pragma unroll
    for (int ks = 0; ks < 4; ++ks) MFV(ks == 0, sm, km[ks], qf[ks]);
    mfma_settle3(sx0, sx1, sm);
    v4u p0, p1;
    softmax_frags(sx0, sx1, sm, ok0, ok1, g < 2, p0, p1);
    {
      f32x4 o2[8];
#pragma unroll
      for (int m = 0; m < 4; ++m) {
        const v4u vm0 = {vm2[2 * m].x, vm2[2 * m].y, 0u, 0u}, vm1 = {vm2[2 * m + 1].x, vm2[2 * m + 1].y, 0u, 0u};
        mfma_pv(o2[2 * m], o2[2 * m + 1], vf[2 * m], vf[2 * m + 1], vm0, vm1, p0, p1);
      }
      mfma_settle(o2[0], o2[1], o2[2], o2[3]);
      mfma_settle(o2[4], o2[5], o2[6], o2[7]);
#pragma unroll
      for (int m = 0; m < 4; ++m) *(LAS v4u*)(EX + (w * 4 + m) * 1024 + lane * 16) = pack8(o2[2 * m], o2[2 * m + 1]);
    }
    have_qkv = __builtin_amdgcn_readfirstlane(nq);
    loaded = s + 1 < NSEG && have_qkv >= (unsigned)(s + 2);
    if (loaded) {
      const unsigned ib = qbase + (unsigned)((s + 1) * NH * 24 * 1024);
#pragma unroll
      for (int ks = 0; ks < 4; ++ks) qf[ks] = ld16s(rs, l16 + (unsigned)(ks * 1024), ib + (unsigned)(tt * 4096));
#pragma unroll
      for (int i = 0; i < 8; ++i) kf[i] = ld16s(rs, l16 + (unsigned)((i & 3) * 1024), ib + (unsigned)(8192 + (i & 4) * 1024));
#pragma unroll
      for (int i = 0; i < 8; ++i) vf[i] = ld16s(rs, l16 + (unsigned)((i & 3) * 1024), ib + (unsigned)(16384 + (i & 4) * 1024));
    }
    SB();
    __syncthreads();
    f32x4 acc[8][2];
    {
      v4u of[4];
#pragma unroll
      for (int i = 0; i < 4; ++i) of[i] = *(const LAS v4u*)(EX + i * 1024 + lane * 16);
      SB();
#pragma unroll
      for (int i = 0; i < 16; ++i) {
        const int h2 = i >> 3, t2 = (i >> 2) & 1, ks = i & 3;
        const bool first = h2 == 0 && ks == 0;
        const v4u x = of[i & 3];
#pragma unroll
        for (int dt = 0; dt < 4; ++dt) MFA(first, acc[dt][t2], wo[h2][dt][ks], x);
        SB();
        if (i >= 1 && i + 3 < 16) of[(i + 3) & 3] = *(const LAS v4u*)(EX + (i + 3) * 1024 + lane * 16);
#pragma unroll
        for (int dt = 4; dt < 8; ++dt) MFA(first, acc[dt][t2], wo[h2][dt][ks], x);
        SB();
      }
    }
#pragma unroll
    for (int t2 = 0; t2 < 2; ++t2) {
      mfma_settle(acc[0][t2], acc[1][t2], acc[2][t2], acc[3][t2]);
      mfma_settle(acc[4][t2], acc[5][t2], acc[6][t2], acc[7][t2]);
    }
    STAMP0(s, 2);
    partial_put(TR, acc, w, lane);
    __syncthreads();
    partial_store(rs, (unsigned)(OFF_PO + (size_t)(s * NATT + pp) * 32 * KB), TR, w, lane);
    const unsigned nk = ld4(rs, cnt_off(K_KM, c, h));
    DRAIN();
    have_km = __builtin_amdgcn_readfirstlane(nk);
    __syncthreads();
    if (tid == 0) st4(rs, cnt_off(K_ATT, c, pp), (unsigned)(s + 1));
    STAMP0(s, 3);
  }
}

__device__ __forceinline__ float gelu_tanh(float x) {
  const float z = x * fmaf(x * x, -2.0f * 0.7978845608028654f * 0.044715f * 1.4426950408889634f, -2.0f * 0.7978845608028654f * 1.4426950408889634f);
  return x * __builtin_amdgcn_rcpf(1.0f + __builtin_amdgcn_exp2f(z));
}
__device__ __forceinline__ unsigned geglu2(float g0, float g1, float u0, float u1) {
  const unsigned gp = pack2(g0, g1), up = pack2(u0, u1);
  const unsigned ap = pack2(gelu_tanh(bflo(gp)), gelu_tanh(bfhi(gp)));
  return pack2(bflo(ap) * bflo(up), bfhi(ap) * bfhi(up));
}
__device__ __forceinline__ void role_ffn(rsrc_t rs, LAS unsigned char* L, int c, int sl) {
  const int tid = threadIdx.x, lane = tid & 63, w = __builtin_amdgcn_readfirstlane(tid >> 6);
  const int g = lane >> 4, r = lane & 15;
  v4u wg[2][16], wu[2][16], wd[8][4];
#pragma unroll
  for (int t = 0; t < 2; ++t)
#pragma unroll
    for (int ks = 0; ks < 16; ++ks) {
      const unsigned o = (unsigned)(((sl * 8 + 2 * w + t) * 16 + ks) * 1024 + lane * 16);
      wg[t][ks] = ld16p(rs, (unsigned)OFF_WG + o);
      wu[t][ks] = ld16p(rs, (unsigned)OFF_WU + o);
    }
#pragma unroll
  for (int dt = 0; dt < 8; ++dt)
#pragma unroll
    for (int ks = 0; ks < 4; ++ks) wd[dt][ks] = ld16p(rs, (unsigned)OFF_WD + (unsigned)(((sl * 32 + 8 * w + dt) * 4 + ks) * 1024 + lane * 16));
  LAS unsigned char* X = L;
  LAS unsigned char* EX = L + IMG;
  LAS unsigned char* TR = L + IMG + 8192;
  const int fb = r * RSTR + g * 16;
  volatile LAS unsigned* RDY = (volatile LAS unsigned*)(L + 2 * IMG + 8192);
  const unsigned xbase = (unsigned)(OFF_XF + (size_t)c * 2 * MB);
  bool staged = false;
  for (int s = 0; s < NSEG; ++s) {
    if (!staged) {
      if (w == 0) wait_cnt<8>(rs, cnt_off(K_X1, c, 0), 32, 2 * sl, (unsigned)(s + 1), lane);
      __syncthreads();
      STAMP0(s, 0);
      v4u v[8];
      rows_load(rs, xbase + (unsigned)s * 32768u, v, w, lane);
      rows_put(X, v, w, lane);
      __syncthreads();
    }
    STAMP0(s, 1);
    f32x4 ag[2][2], au[2][2];
    {
      v4u xf[4];
#pragma unroll
      for (int i = 0; i < 4; ++i) xf[i] = row_frag(X, 0, i, fb);
      SB();
      unsigned hv[4];
#pragma unroll
      for (int i = 0; i < 32; ++i) {
        const int ks = i & 15, tt = i >> 4;
        const v4u x = xf[i & 3];
        MFA(ks == 0, ag[0][tt], wg[0][ks], x);
        MFA(ks == 0, ag[1][tt], wg[1][ks], x);
        SB();
        if (i >= 1 && i + 3 < 32) xf[(i + 3) & 3] = row_frag(X, (i + 3) >> 4, (i + 3) & 15, fb);
        MFA(ks == 0, au[0][tt], wu[0][ks], x);
        MFA(ks == 0, au[1][tt], wu[1][ks], x);
        SB();
        if (i >= 19 && i < 27 && (i & 1)) {
          const int q = (i - 19) >> 1, t = q >> 1, j = (q & 1) * 2;
          hv[q] = geglu2(ag[t][0][j], ag[t][0][j + 1], au[t][0][j], au[t][0][j + 1]);
          SB();
        }
        if (i == 27) { *(LAS v4u*)(EX + w * 1024 + lane * 16) = v4u{hv[0], hv[1], hv[2], hv[3]}; SB(); }
      }
      mfma_settle(ag[0][1], ag[1][1], au[0][1], au[1][1]);
#pragma unroll
      for (int t = 0; t < 2; ++t)
#pragma unroll
        for (int j = 0; j < 4; j += 2) hv[2 * t + (j >> 1)] = geglu2(ag[t][1][j], ag[t][1][j + 1], au[t][1][j], au[t][1][j + 1]);
      *(LAS v4u*)(EX + (4 + w) * 1024 + lane * 16) = v4u{hv[0], hv[1], hv[2], hv[3]};
    }
    unsigned nx = 0;
    if (w == 0) nx = ld4(rs, cnt_off(K_X1, c, 0) + (unsigned)(lane & 31) * 128u);
    __syncthreads();
    f32x4 acc[8][2];
    {
      v4u hf[4];
#pragma unroll
      for (int i = 0; i < 4; ++i) hf[i] = *(const LAS v4u*)(EX + i * 1024 + lane * 16);
      SB();
#pragma unroll
      for (int i = 0; i < 8; ++i) {
        const int tt = i >> 2, ks = i & 3;
        const v4u x = hf[ks];
#pragma unroll
        for (int dt = 0; dt < 4; ++dt) MFV(ks == 0, acc[dt][tt], wd[dt][ks], x);
        SB();
        if (i >= 1 && i + 3 < 8) hf[(i + 3) & 3] = *(const LAS v4u*)(EX + (i + 3) * 1024 + lane * 16);
#pragma unroll
        for (int dt = 4; dt < 8; ++dt) MFV(ks == 0, acc[dt][tt], wd[dt][ks], x);
        SB();
      }
    }
#pragma unroll
    for (int t2 = 0; t2 < 2; ++t2) {
      mfma_settle(acc[0][t2], acc[1][t2], acc[2][t2], acc[3][t2]);
      mfma_settle(acc[4][t2], acc[5][t2], acc[6][t2], acc[7][t2]);
    }
    STAMP0(s, 2);
    partial_put(TR, acc, w, lane);
    if (w == 0) {
      const bool ok = __all(nx >= (unsigned)(s + 2)) && s + 1 < NSEG;
      if (lane == 0) RDY[0] = ok ? 1u : 0u;
    }
    __syncthreads();
    staged = RDY[0] != 0u;
    if (staged) {
      v4u nv[8];
      rows_load(rs, xbase + (unsigned)(s + 1) * 32768u, nv, w, lane);
      partial_store(rs, (unsigned)(OFF_PF + (size_t)(s * NFFN + sl) * 32 * KB), TR, w, lane);
      rows_put(X, nv, w, lane);
    } else {
      partial_store(rs, (unsigned)(OFF_PF + (size_t)(s * NFFN + sl) * 32 * KB), TR, w, lane);
    }
    DRAIN();
    __syncthreads();
    if (tid == 0) st4(rs, cnt_off(K_FFN, c, sl), (unsigned)(s + 1));
    STAMP0(s, 3);
  }
}

template <int NP>
__device__ __forceinline__ void sum_rows(rsrc_t rs, unsigned off, float (&a)[8]) {
  v4u v[NP];
#pragma unroll
  for (int i = 0; i < NP; ++i) v[i] = ld16(rs, off + (unsigned)i * 32768u);
  SB();
#pragma unroll
  for (int j = 0; j < 8; ++j) a[j] = 0.f;
#pragma unroll
  for (int i = 0; i < NP; ++i) {
    a[0] += bflo(v[i].x); a[1] += bfhi(v[i].x); a[2] += bflo(v[i].y); a[3] += bfhi(v[i].y);
    a[4] += bflo(v[i].z); a[5] += bfhi(v[i].z); a[6] += bflo(v[i].w); a[7] += bfhi(v[i].w);
  }
}
__device__ __forceinline__ void role_red(const Params& p, rsrc_t rs, LAS unsigned char* L, int t) {
  const int tid = threadIdx.x, lane = tid & 63, w = __builtin_amdgcn_readfirstlane(tid >> 6);
  const int c = w >> 1, ffn = w & 1;
  for (int s = 0; s < NSEG; ++s) {
    float a[8];
    if (!ffn) {
      wait_cnt<8>(rs, cnt_off(K_ATT, c, 0), NATT, t & 15, (unsigned)(s + 1), lane);
      STAMP(s, 2 * w);
      sum_rows<NATT>(rs, (unsigned)(OFF_PO + (size_t)s * NATT * 32 * KB) + (unsigned)(t * 1024 + lane * 16), a);
    } else {
      wait_cnt<8>(rs, cnt_off(K_FFN, c, 0), NFFN, t % NFFN, (unsigned)(s + 1), lane);
      STAMP(s, 2 * w);
      sum_rows<NFFN>(rs, (unsigned)(OFF_PF + (size_t)s * NFFN * 32 * KB) + (unsigned)(t * 1024 + lane * 16), a);
    }
    v4u xo;
    if (w == 0) xo = *(const v4u*)(p.x + (size_t)(s * SEG + t) * DM + lane * 8);
    else xo = *(volatile LAS v4u*)(L + s * 1024 + lane * 16);
    float x[8] = {bflo(xo.x), bfhi(xo.x), bflo(xo.y), bfhi(xo.y), bflo(xo.z), bfhi(xo.z), bflo(xo.w), bfhi(xo.w)};
#pragma unroll
    for (int j = 0; j < 8; ++j) x[j] = bf16r(x[j] + bf16r(a[j]));
    v4u xn; xn.x = pack2(x[0], x[1]); xn.y = pack2(x[2], x[3]); xn.z = pack2(x[4], x[5]); xn.w = pack2(x[6], x[7]);
    if (w == 3) {
      *(v4u*)(p.out + (size_t)(s * SEG + t) * DM + lane * 8) = xn;
    } else {
      *(volatile LAS v4u*)(L + s * 1024 + lane * 16) = xn;
      float y[8];
      layernorm8(x, y);
      v4u o; o.x = pack2(y[0], y[1]); o.y = pack2(y[2], y[3]); o.z = pack2(y[4], y[5]); o.w = pack2(y[6], y[7]);
      const unsigned dst = (ffn ? (unsigned)(OFF_XA + 2 * MB) : (unsigned)(OFF_XF + (size_t)c * 2 * MB)) + (unsigned)((s * SEG + t) * 1024 + lane * 16);
      st16(rs, dst, o);
      LDSWAIT();
      DRAIN();
      const unsigned f = ffn ? cnt_off(K_X2, 0, t) : cnt_off(K_X1, c, t);
      if (lane == 0) st4(rs, f, (unsigned)(s + 1));
    }
    STAMP(s, 2 * w + 1);
  }
}

__global__ void __launch_bounds__(256, 1) k_main(Params p) {
  __shared__ __attribute__((aligned(16))) unsigned char lds[LDS_BYTES];
  LAS unsigned char* L = (LAS unsigned char*)lds;
  const rsrc_t rs = __builtin_amdgcn_make_buffer_rsrc((void*)p.ws, 0, (int)WS_BYTES, 0x00020000);
  const int b = blockIdx.x;
  if (b < 64) role_chain(p, rs, L, b >> 5, b & 31);
  else if (b < 96) role_creduce(p, rs, L, (b - 64) >> 4, (b - 64) & 15);
  else if (b < 160) role_qkv(rs, L, (b - 96) >> 5, (b - 96) & 31);
  else if (b < 192) role_att(rs, L, (b - 160) >> 4, (b - 160) & 15);
  else if (b < 220) role_ffn(rs, L, (b - 192) / NFFN, (b - 192) % NFFN);
  else if (b < 252) role_red(p, rs, L, b - 220);
}

extern "C" void kernel_launch(void* const* d_in, const int* in_sizes, int n_in,
                              void* d_out, int out_size, void* d_ws, size_t ws_size,
                              hipStream_t stream) {
  PrepParams pp;
  pp.x = (const u16*)d_in[0];
  pp.wq = (const u16*)d_in[2];
  pp.wk = (const u16*)d_in[3];
  pp.wv = (const u16*)d_in[4];
  pp.wo = (const u16*)d_in[5];
  pp.wg = (const u16*)d_in[8];
  pp.wu = (const u16*)d_in[9];
  pp.wd = (const u16*)d_in[10];
  pp.ws = (unsigned char*)d_ws;
  k_prep<<<NJ_ALL, 256, 0, stream>>>(pp);
  Params p;
  p.x = (const u16*)d_in[0];
  p.mem0 = (const u16*)d_in[1];
  p.out = (u16*)d_out;
  p.ws = (unsigned char*)d_ws;
  k_main<<<252, 256, 0, stream>>>(p);
}
```

```cpp
#include <hip/hip_runtime.h>
#include <hip/hip_bf16.h>
#include <stdint.h>

#define GAS __attribute__((address_space(1)))
#define LAS __attribute__((address_space(3)))
typedef unsigned v4u __attribute__((ext_vector_type(4)));
typedef unsigned v2u __attribute__((ext_vector_type(2)));
typedef float f32x4 __attribute__((ext_vector_type(4)));
typedef float f32x2 __attribute__((ext_vector_type(2)));
typedef short bf16x8 __attribute__((ext_vector_type(8)));
typedef __bf16 bf16x2 __attribute__((ext_vector_type(2)));
typedef GAS unsigned gu32;
typedef unsigned short u16;
#define RLX_AGENT __ATOMIC_RELAXED, __HIP_MEMORY_SCOPE_AGENT
#define SB() __builtin_amdgcn_sched_barrier(0)
#define DRAIN() asm volatile("s_waitcnt vmcnt(0)" ::: "memory")
#define LDSWAIT() asm volatile("s_waitcnt lgkmcnt(0)" ::: "memory")

constexpr int S_TOK = 2048, DM = 512, NH = 32, DH = 128, NMEM = 8, SEG = 32, NSEG = 64, DFF = 1792;
constexpr int NFFN = 14, NATT = 16;

constexpr size_t KB = 1024, MB = 1024 * 1024;
constexpr size_t OFF_ABORT = 0;
enum { K_QKV = 0, K_KM = 1, K_ATT = 2, K_X1 = 3, K_FFN = 4, K_X2 = 5, K_COL = 6 };
constexpr size_t OFF_CNT = 4 * KB;
__host__ __device__ constexpr unsigned cnt_off(int kind, int c, int idx) { return (unsigned)(OFF_CNT + (size_t)((kind * 2 + c) * 32 + idx) * 128); }
constexpr size_t ZERO_BYTES = 64 * KB;
constexpr size_t ROW = 32 * KB;
constexpr size_t OFF_WQKV = 2 * MB;
constexpr size_t OFF_WO = OFF_WQKV + 12 * MB;
constexpr size_t OFF_WG = OFF_WO + 4 * MB;
constexpr size_t OFF_WU = OFF_WG + NFFN * 128 * KB;
constexpr size_t OFF_WD = OFF_WU + NFFN * 128 * KB;
constexpr size_t OFF_XA = 24 * MB;
constexpr size_t OFF_XF = OFF_XA + 4 * MB;
constexpr size_t OFF_KVM = OFF_XF + 4 * MB;
constexpr size_t OFF_QKVF = OFF_KVM + 16 * MB;
constexpr size_t OFF_PO = OFF_QKVF + 96 * MB;
constexpr size_t OFF_PF = OFF_PO + 32 * MB;
constexpr size_t OFF_P = OFF_PF + 28 * MB;
constexpr size_t CR_BLK = 16 * ROW, P_CHAIN = 16 * CR_BLK, M_ROW0 = 8 * ROW, M_SLOT = 2 * ROW;
constexpr size_t WS_BYTES = OFF_P + 2 * P_CHAIN + CR_BLK;
__device__ __forceinline__ unsigned p_region(const unsigned char* ws, int c) {
  return (unsigned)OFF_P + (unsigned)((0u - (unsigned)(uintptr_t)ws - (unsigned)OFF_P) & (unsigned)(CR_BLK - 1)) + (unsigned)c * (unsigned)P_CHAIN;
}
static_assert(OFF_CNT + 14 * 32 * 128 <= ZERO_BYTES, "flags");
static_assert(OFF_WD + NFFN * 128 * KB <= OFF_XA, "weights");
static_assert(WS_BYTES <= 256 * MB, "workspace");

constexpr unsigned kSpinCap = 1u << 17;
#define POLL 3
#define CAL_WAIT 0
#define CAL_N 2
#define CH_SLEEP 0
#define CR_SLEEP 56
constexpr int LDS_BYTES = 163840;

#define STAMP(s, i) do {} while (0)
#define STAMP0(s, i) do {} while (0)

#define TICKDECL do {} while (0)
#define TICKDUMP do {} while (0)
struct Params {
  const u16* x;
  const u16* mem0;
  u16* out;
  unsigned char* ws;
};
struct PrepParams {
  const u16* x;
  const u16* wq;
  const u16* wk;
  const u16* wv;
  const u16* wo;
  const u16* wg;
  const u16* wu;
  const u16* wd;
  unsigned char* ws;
};

__device__ __forceinline__ unsigned xcc_id() { return (unsigned)__builtin_amdgcn_s_getreg(20 | (3 << 11)) & 7u; }
__device__ __forceinline__ unsigned pack2(float a, float b) {
  f32x2 v = {a, b};
  bf16x2 r = __builtin_convertvector(v, bf16x2);
  return __builtin_bit_cast(unsigned, r);
}
__device__ __forceinline__ float bf16r(float a) { return __uint_as_float(pack2(a, 0.f) << 16); }
__device__ __forceinline__ float bflo(unsigned u) { return __uint_as_float(u << 16); }
__device__ __forceinline__ float bfhi(unsigned u) { return __uint_as_float(u & 0xffff0000u); }
#define MFMA "v_mfma_f32_16x16x32_bf16 "
template <bool FIRST> __device__ __forceinline__ void mfa(f32x4& c, v4u a, v4u b) {
  if (FIRST) asm(MFMA "%0, %1, %2, 0" : "=&v"(c) : "a"(a), "v"(b));
  else asm(MFMA "%0, %1, %2, %0" : "+v"(c) : "a"(a), "v"(b));
}
template <bool FIRST> __device__ __forceinline__ void mfv(f32x4& c, v4u a, v4u b) {
  if (FIRST) asm(MFMA "%0, %1, %2, 0" : "=&v"(c) : "v"(a), "v"(b));
  else asm(MFMA "%0, %1, %2, %0" : "+v"(c) : "v"(a), "v"(b));
}
#define MFA(first, c, a, b) do { if (first) mfa<true>(c, a, b); else mfa<false>(c, a, b); } while (0)
#define MFV(first, c, a, b) do { if (first) mfv<true>(c, a, b); else mfv<false>(c, a, b); } while (0)
__device__ __forceinline__ void mfma4a(f32x4& c0, f32x4& c1, f32x4& c2, f32x4& c3, v4u a0, v4u a1, v4u a2, v4u a3, v4u b) {
  asm("s_nop 1\n\t"
      "v_mfma_f32_16x16x32_bf16 %0, %4, %8, %0\n\t"
      "v_mfma_f32_16x16x32_bf16 %1, %5, %8, %1\n\t"
      "v_mfma_f32_16x16x32_bf16 %2, %6, %8, %2\n\t"
      "v_mfma_f32_16x16x32_bf16 %3, %7, %8, %3"
      : "+v"(c0), "+v"(c1), "+v"(c2), "+v"(c3)
      : "a"(a0), "a"(a1), "a"(a2), "a"(a3), "v"(b));
}
__device__ __forceinline__ void mfma6a(f32x4& c0, f32x4& c1, f32x4& c2, f32x4& c3, f32x4& c4, f32x4& c5,
                                       v4u a0, v4u a1, v4u a2, v4u a3, v4u b4, v4u b5, v4u b) {
  asm("s_nop 1\n\t"
      MFMA "%0, %6, %12, %0\n\t"
      MFMA "%1, %7, %12, %1\n\t"
      MFMA "%2, %8, %12, %2\n\t"
      MFMA "%3, %9, %12, %3\n\t"
      MFMA "%4, %12, %10, %4\n\t"
      MFMA "%5, %12, %11, %5"
      : "+v"(c0), "+v"(c1), "+v"(c2), "+v"(c3), "+v"(c4), "+v"(c5)
      : "a"(a0), "a"(a1), "a"(a2), "a"(a3), "v"(b4), "v"(b5), "v"(b));
}
__device__ __forceinline__ void mfma2v(f32x4& c0, f32x4& c1, v4u a, v4u b0, v4u b1) {
  asm("s_nop 1\n\t"
      MFMA "%0, %2, %3, %0\n\t"
      MFMA "%1, %2, %4, %1"
      : "+v"(c0), "+v"(c1)
      : "v"(a), "v"(b0), "v"(b1));
}
__device__ __forceinline__ void mfma4v(f32x4& c0, f32x4& c1, f32x4& c2, f32x4& c3, v4u a0, v4u a1, v4u a2, v4u a3, v4u b) {
  asm("s_nop 1\n\t"
      MFMA "%0, %4, %8, %0\n\t"
      MFMA "%1, %5, %8, %1\n\t"
      MFMA "%2, %6, %8, %2\n\t"
      MFMA "%3, %7, %8, %3"
      : "+v"(c0), "+v"(c1), "+v"(c2), "+v"(c3)
      : "v"(a0), "v"(a1), "v"(a2), "v"(a3), "v"(b));
}
__device__ __forceinline__ void mfma3v(f32x4& c0, f32x4& c1, f32x4& c2, v4u a0, v4u a1, v4u a2, v4u b) {
  asm("s_nop 1\n\t"
      MFMA "%0, %3, %6, %0\n\t"
      MFMA "%1, %4, %6, %1\n\t"
      MFMA "%2, %5, %6, %2"
      : "+v"(c0), "+v"(c1), "+v"(c2)
      : "v"(a0), "v"(a1), "v"(a2), "v"(b));
}
__device__ __forceinline__ void mfma_pv(f32x4& c0, f32x4& c1, v4u a0, v4u a1, v4u m, v4u p0, v4u pl, v4u ph) {
  asm("s_nop 1\n\t"
      MFMA "%0, %2, %5, 0\n\t"
      MFMA "%1, %3, %5, 0\n\t"
      MFMA "%0, %4, %6, %0\n\t"
      MFMA "%1, %4, %7, %1"
      : "=&v"(c0), "=&v"(c1)
      : "v"(a0), "v"(a1), "v"(m), "v"(p0), "v"(pl), "v"(ph));
}
__device__ __forceinline__ void mfma_k4(f32x4& c, v4u a0, v4u a1, v4u a2, v4u a3, v4u b0, v4u b1, v4u b2, v4u b3) {
  asm("s_nop 1\n\t"
      MFMA "%0, %1, %5, 0\n\t"
      MFMA "%0, %2, %6, %0\n\t"
      MFMA "%0, %3, %7, %0\n\t"
      MFMA "%0, %4, %8, %0"
      : "=&v"(c)
      : "v"(a0), "v"(a1), "v"(a2), "v"(a3), "v"(b0), "v"(b1), "v"(b2), "v"(b3));
}
__device__ __forceinline__ void mfma_k2(f32x4& c, v4u a, v4u b0, v4u b1) {
  asm("s_nop 1\n\t"
      MFMA "%0, %1, %2, 0\n\t"
      MFMA "%0, %1, %3, %0"
      : "=&v"(c)
      : "v"(a), "v"(b0), "v"(b1));
}
__device__ __forceinline__ void mfma_settle2(f32x4& c0, f32x4& c1) { asm("s_nop 7\n\ts_nop 1" : "+v"(c0), "+v"(c1)); }
__device__ __forceinline__ void mfma_settle3(f32x4& c0, f32x4& c1, f32x4& c2) { asm("s_nop 7\n\ts_nop 1" : "+v"(c0), "+v"(c1), "+v"(c2)); }
__device__ __forceinline__ void mfma_settle(f32x4& c0, f32x4& c1, f32x4& c2, f32x4& c3) {
  asm("s_nop 7\n\ts_nop 1" : "+v"(c0), "+v"(c1), "+v"(c2), "+v"(c3));
}
__device__ __forceinline__ v4u pack8(f32x4 a, f32x4 b) {
  v4u r;
  r.x = pack2(a.x, a.y); r.y = pack2(a.z, a.w); r.z = pack2(b.x, b.y); r.w = pack2(b.z, b.w);
  return r;
}
__device__ __forceinline__ float xmax(float x) {
  auto s = __builtin_amdgcn_permlane16_swap(__float_as_uint(x), __float_as_uint(x), false, false);
  x = fmaxf(__uint_as_float(s[0]), __uint_as_float(s[1]));
  auto t = __builtin_amdgcn_permlane32_swap(__float_as_uint(x), __float_as_uint(x), false, false);
  return fmaxf(__uint_as_float(t[0]), __uint_as_float(t[1]));
}
__device__ __forceinline__ float xsum(float x) {
  auto s = __builtin_amdgcn_permlane16_swap(__float_as_uint(x), __float_as_uint(x), false, false);
  x = __uint_as_float(s[0]) + __uint_as_float(s[1]);
  auto t = __builtin_amdgcn_permlane32_swap(__float_as_uint(x), __float_as_uint(x), false, false);
  return __uint_as_float(t[0]) + __uint_as_float(t[1]);
}
__device__ __forceinline__ unsigned ror8(unsigned x) {
  return (unsigned)__builtin_amdgcn_update_dpp(0, (int)x, 0x128, 0xf, 0xf, false);
}
__device__ __forceinline__ float ror8f(float x) { return __uint_as_float(ror8(__float_as_uint(x))); }
template <int CTRL> __device__ __forceinline__ float dppf(float x) {
  return __uint_as_float((unsigned)__builtin_amdgcn_update_dpp(0, (int)__float_as_uint(x), CTRL, 0xf, 0xf, false));
}
__device__ __forceinline__ float wave_sum(float x) {
  x += dppf<0x121>(x); x += dppf<0x122>(x); x += dppf<0x124>(x); x += dppf<0x128>(x);
  return xsum(x);
}
typedef __amdgpu_buffer_rsrc_t rsrc_t;
__device__ __forceinline__ v4u ld16(rsrc_t rs, unsigned off) { return __builtin_amdgcn_raw_buffer_load_b128(rs, (int)off, 0, 16); }
__device__ __forceinline__ v4u ld16s(rsrc_t rs, unsigned voff, unsigned soff) { return __builtin_amdgcn_raw_buffer_load_b128(rs, (int)voff, (int)soff, 16); }
__device__ __forceinline__ v2u ld8(rsrc_t rs, unsigned off) { return __builtin_amdgcn_raw_buffer_load_b64(rs, (int)off, 0, 16); }
__device__ __forceinline__ v2u ld8s(rsrc_t rs, unsigned voff, unsigned soff) { return __builtin_amdgcn_raw_buffer_load_b64(rs, (int)voff, (int)soff, 16); }
__device__ __forceinline__ unsigned ld4(rsrc_t rs, unsigned off) { return __builtin_amdgcn_raw_buffer_load_b32(rs, (int)off, 0, 16); }
__device__ __forceinline__ v4u ld16p(rsrc_t rs, unsigned off) { return __builtin_amdgcn_raw_buffer_load_b128(rs, (int)off, 0, 0); }
__device__ __forceinline__ void st16(rsrc_t rs, unsigned off, v4u v) { __builtin_amdgcn_raw_buffer_store_b128(v, rs, (int)off, 0, 16); }
__device__ __forceinline__ void st16s(rsrc_t rs, unsigned voff, unsigned soff, v4u v) { __builtin_amdgcn_raw_buffer_store_b128(v, rs, (int)voff, (int)soff, 16); }
__device__ __forceinline__ void st8(rsrc_t rs, unsigned off, v2u v) { __builtin_amdgcn_raw_buffer_store_b64(v, rs, (int)off, 0, 16); }
__device__ __forceinline__ void st4(rsrc_t rs, unsigned off, unsigned v) { __builtin_amdgcn_raw_buffer_store_b32(v, rs, (int)off, 0, 16); }

__device__ __forceinline__ bool spin_fail(unsigned& spins, rsrc_t rs) {
  ++spins;
  if (spins > kSpinCap) { st4(rs, (unsigned)OFF_ABORT, 1u); return true; }
  if ((spins & 127u) == 0u) return __builtin_amdgcn_readfirstlane(ld4(rs, (unsigned)OFF_ABORT)) != 0u;
  return false;
}
template <int SLEEP>
__device__ __forceinline__ void wait_cnt(rsrc_t rs, unsigned off, int n, int probe, unsigned want, int lane) {
  unsigned spins = 0;
  for (;;) {
    const unsigned v = ld4(rs, off + (unsigned)probe * 128u);
    if (__builtin_amdgcn_readfirstlane(v) >= want) break;
    if (spin_fail(spins, rs)) break;
    __builtin_amdgcn_s_sleep(SLEEP);
  }
  if (n > 1) {
    for (;;) {
      const unsigned v = ld4(rs, off + (unsigned)(lane < n ? lane : 0) * 128u);
      if (__all(v >= want)) break;
      if (spin_fail(spins, rs)) break;
      __builtin_amdgcn_s_sleep(2);
    }
  }
  __builtin_amdgcn_fence(__ATOMIC_ACQUIRE, "wavefront");
}

__device__ __forceinline__ unsigned wait_table(rsrc_t rs, int c, int lane) {
  unsigned spins = 0, v;
  for (;;) {
    v = ld4(rs, cnt_off(K_COL, c, 0) + (unsigned)(lane & 15) * 128u);
    if (__all(v != 0u)) break;
    if (spin_fail(spins, rs)) break;
    __builtin_amdgcn_s_sleep(8);
  }
  return v - 1u;
}
__device__ __forceinline__ unsigned mem_copy(unsigned tab, unsigned x) {
  const unsigned long long m = __ballot(((tab >> 3) & 7u) == x) & 0xffffull;
  const int own = m == 0ull ? 0 : __builtin_ctzll(m);
  return (unsigned)own * (unsigned)CR_BLK + (unsigned)M_ROW0 + ((unsigned)__builtin_amdgcn_readlane(tab, own) & 7u) * 4096u;
}

__device__ __forceinline__ void layernorm8(const float (&x)[8], float (&y)[8]) {
  float s = 0.f;
#pragma unroll
  for (int j = 0; j < 8; ++j) s += x[j];
  const float mean = wave_sum(s) * (1.0f / DM);
  float q = 0.f;
#pragma unroll
  for (int j = 0; j < 8; ++j) { const float d = x[j] - mean; q += d * d; }
  const float var = wave_sum(q) * (1.0f / DM);
  const float m = bf16r(mean);
  const float rden = 1.0f / bf16r(sqrtf(bf16r(bf16r(var) + 1e-5f)));
#pragma unroll
  for (int j = 0; j < 8; ++j) y[j] = bf16r(x[j] - m) * rden;
}

constexpr int NJ_QKV = 3 * 16 * 32;
constexpr int NJ_WO = 32 * 4 * 4;
constexpr int NJ_GU = 2 * 16 * NFFN;
constexpr int NJ_WD = NFFN * 4 * 4;
constexpr int NJ_LN = S_TOK / 4;
constexpr int NJ_INIT = 256;
constexpr int NJ_ALL = NJ_QKV + NJ_WO + NJ_GU + NJ_WD + NJ_LN + NJ_INIT;

__global__ void __launch_bounds__(256) k_prep(PrepParams p) {
  __shared__ __attribute__((aligned(16))) u16 tile[32][136];
  const int tid = threadIdx.x, lane = tid & 63, w = tid >> 6;
  int job = blockIdx.x;
  if (job < NJ_QKV + NJ_WO + NJ_GU + NJ_WD) {
    const u16* src; int ld; bool pi; size_t dst; size_t tstride;
    if (job < NJ_QKV) {
      const int m = job / 512, ks = (job >> 5) & 15, h = job & 31;
      const u16* W = m == 0 ? p.wq : (m == 1 ? p.wk : p.wv);
      src = W + (size_t)(32 * ks) * (NH * DH) + h * DH; ld = NH * DH; pi = false;
      dst = OFF_WQKV + ((size_t)(m * 32 + h) * 8 * 16 + ks) * KB; tstride = 16 * KB;
    } else if (job < NJ_QKV + NJ_WO) {
      job -= NJ_QKV;
      const int h = job >> 4, ks = (job >> 2) & 3, nb = job & 3;
      src = p.wo + (size_t)(h * DH + 32 * ks) * DM + nb * 128; ld = DM; pi = true;
      dst = OFF_WO + ((size_t)(h * 32 + nb * 8) * 4 + ks) * KB; tstride = 4 * KB;
    } else if (job < NJ_QKV + NJ_WO + NJ_GU) {
      job -= NJ_QKV + NJ_WO;
      const int m = job / (16 * NFFN), r = job % (16 * NFFN), ks = r / NFFN, sl = r % NFFN;
      src = (m == 0 ? p.wg : p.wu) + (size_t)(32 * ks) * DFF + sl * 128; ld = DFF; pi = false;
      dst = (m == 0 ? OFF_WG : OFF_WU) + ((size_t)sl * 8 * 16 + ks) * KB; tstride = 16 * KB;
    } else {
      job -= NJ_QKV + NJ_WO + NJ_GU;
      const int sl = job >> 4, ks = (job >> 2) & 3, nb = job & 3;
      src = p.wd + (size_t)(sl * 128 + 32 * ks) * DM + nb * 128; ld = DM; pi = true;
      dst = OFF_WD + ((size_t)(sl * 32 + nb * 8) * 4 + ks) * KB; tstride = 4 * KB;
    }
#pragma unroll
    for (int it = 0; it < 2; ++it) {
      const int idx = it * 256 + tid, r = idx >> 4, c = idx & 15;
      const v4u v = *(const v4u*)(src + (size_t)r * ld + c * 8);
      *(v4u*)&tile[r][c * 8] = v;
    }
    __syncthreads();
    const int g = lane >> 4, r = lane & 15;
#pragma unroll
    for (int it = 0; it < 2; ++it) {
      const int t = w * 2 + it;
      unsigned e[8];
#pragma unroll
      for (int j = 0; j < 8; ++j) {
        const int k = pi ? (16 * (j >> 2) + 4 * g + (j & 3)) : (8 * g + j);
        e[j] = tile[k][pi ? 32 * (t >> 1) + 8 * (r >> 2) + 4 * (t & 1) + (r & 3) : 16 * t + r];
      }
      v4u o; o.x = e[0] | (e[1] << 16); o.y = e[2] | (e[3] << 16); o.z = e[4] | (e[5] << 16); o.w = e[6] | (e[7] << 16);
      *(v4u*)(p.ws + dst + (size_t)t * tstride + lane * 16) = o;
    }
    return;
  }
  job -= NJ_QKV + NJ_WO + NJ_GU + NJ_WD;
  if (job < NJ_LN) {
    const int row = job * 4 + w;
    const v4u v = *(const v4u*)(p.x + (size_t)row * DM + lane * 8);
    float x[8] = {bflo(v.x), bfhi(v.x), bflo(v.y), bfhi(v.y), bflo(v.z), bfhi(v.z), bflo(v.w), bfhi(v.w)}, y[8];
    layernorm8(x, y);
    v4u o; o.x = pack2(y[0], y[1]); o.y = pack2(y[2], y[3]); o.z = pack2(y[4], y[5]); o.w = pack2(y[6], y[7]);
    *(v4u*)(p.ws + OFF_XA + (size_t)row * 1024 + lane * 16) = o;
    return;
  }
  job -= NJ_LN;
  {
    const v4u z = {0u, 0u, 0u, 0u};
    for (size_t i = (size_t)job * 256 + tid; i < ZERO_BYTES / 16; i += (size_t)NJ_INIT * 256) *(v4u*)(p.ws + i * 16) = z;
    *(v4u*)(p.ws + p_region(p.ws, 0) + (size_t)(job >> 3) * CR_BLK + (job & 7) * 4096 + tid * 16) = z;
  }
}

constexpr float kScale = 0.08838834764831845f;
constexpr float kNegBig = -3.0e38f;
constexpr float kLog2e = 1.4426950408889634f;

__device__ __forceinline__ void softmax_frags(f32x4 sx0, f32x4 sx1, f32x4 sm, unsigned ok0, unsigned ok1, bool okm, v4u& p0, v4u& p1) {
  unsigned u[6] = {pack2(sx0.x, sx0.y), pack2(sx0.z, sx0.w), pack2(sx1.x, sx1.y), pack2(sx1.z, sx1.w), pack2(sm.x, sm.y), pack2(sm.z, sm.w)};
  float a[12];
#pragma unroll
  for (int j = 0; j < 6; ++j) {
    u[j] = pack2(bflo(u[j]) * kScale, bfhi(u[j]) * kScale);
    a[2 * j] = bflo(u[j]); a[2 * j + 1] = bfhi(u[j]);
  }
#pragma unroll
  for (int r = 0; r < 4; ++r) {
    a[r] = ((ok0 >> r) & 1u) ? a[r] : kNegBig;
    a[4 + r] = ((ok1 >> r) & 1u) ? a[4 + r] : kNegBig;
    a[8 + r] = okm ? a[8 + r] : kNegBig;
  }
  float mx = fmaxf(fmaxf(fmaxf(a[0], a[1]), fmaxf(a[2], a[3])), fmaxf(fmaxf(a[4], a[5]), fmaxf(a[6], a[7])));
  mx = fmaxf(mx, fmaxf(fmaxf(a[8], a[9]), fmaxf(a[10], a[11])));
  mx = xmax(mx);
  const float nm = -mx * kLog2e;
#pragma unroll
  for (int j = 0; j < 12; ++j) a[j] = __builtin_amdgcn_exp2f(fmaf(a[j], kLog2e, nm));
  float sum = ((a[0] + a[1]) + (a[2] + a[3])) + ((a[4] + a[5]) + (a[6] + a[7])) + ((a[8] + a[9]) + (a[10] + a[11]));
  sum = xsum(sum);
  const float inv = __builtin_amdgcn_rcpf(sum);
  p0.x = pack2(a[0] * inv, a[1] * inv); p0.y = pack2(a[2] * inv, a[3] * inv);
  p0.z = pack2(a[4] * inv, a[5] * inv); p0.w = pack2(a[6] * inv, a[7] * inv);
  p1.x = pack2(a[8] * inv, a[9] * inv); p1.y = pack2(a[10] * inv, a[11] * inv);
  p1.z = 0u; p1.w = 0u;
}

__device__ __forceinline__ void rows_load(rsrc_t rs, unsigned goff, v4u (&v)[8], int w, int lane) {
#pragma unroll
  for (int i = 0; i < 8; ++i) v[i] = ld16s(rs, (unsigned)lane * 16u + (unsigned)(i & 3) * 1024u, goff + (unsigned)(8 * w + (i & 4)) * 1024u);
  SB();
}
constexpr int RSTR = 1040;
constexpr int IMG = 32 * RSTR;
__device__ __forceinline__ void rows_put(LAS unsigned char* L, const v4u (&v)[8], int w, int lane) {
#pragma unroll
  for (int i = 0; i < 8; ++i) *(LAS v4u*)(L + (8 * w + i) * RSTR + lane * 16) = v[i];
}
__device__ __forceinline__ v4u row_frag(const LAS unsigned char* L, int tt, int ks, int fb) {
  return *(const LAS v4u*)(L + fb + ks * 64 + tt * 16 * RSTR);
}
__device__ __forceinline__ void partial_put(LAS unsigned char* L, const f32x4 (&acc)[8][2], int tt, int w, int lane) {
  LAS unsigned char* B = L + (16 * tt + (lane & 15)) * RSTR + w * 256 + (lane >> 4) * 16;
#pragma unroll
  for (int m = 0; m < 4; ++m) *(LAS v4u*)(B + m * 64) = pack8(acc[2 * m][tt], acc[2 * m + 1][tt]);
}
__device__ __forceinline__ v4u partial_row(const LAS unsigned char* L, int tt, int k, int w, int lane) {
  return *(const LAS v4u*)(L + (16 * tt + 4 * w + k) * RSTR + lane * 16);
}
__device__ __forceinline__ void partial_row_store(rsrc_t rs, unsigned goff, v4u v, int tt, int k, int w, int lane) {
  st16s(rs, (unsigned)lane * 16u + (unsigned)k * 1024u, goff + (unsigned)(16 * tt + 4 * w) * 1024u, v);
}
__device__ __forceinline__ void partial_store(rsrc_t rs, unsigned goff, const LAS unsigned char* L, int tt, int w, int lane) {
  v4u v[4];
#pragma unroll
  for (int k = 0; k < 4; ++k) v[k] = partial_row(L, tt, k, w, lane);
#pragma unroll
  for (int k = 0; k < 4; ++k) partial_row_store(rs, goff, v[k], tt, k, w, lane);
  SB();
}

constexpr int CH_KV = 0, CH_MEM = 16384, CH_EX = CH_MEM + 8192, CH_WV = CH_EX + 6144, CH_FL = CH_WV + 131072 + 2032;
__device__ __forceinline__ void role_chain(const Params& p, rsrc_t rs, LAS unsigned char* L, int c, int h) {
  const int tid = threadIdx.x, lane = tid & 63, w = __builtin_amdgcn_readfirstlane(tid >> 6);
  const int g = lane >> 4, r = lane & 15, r8 = lane & 7;
  v4u wq[2][16], wk[2][16], wo[8][4];
#pragma unroll
  for (int t = 0; t < 2; ++t)
#pragma unroll
    for (int ks = 0; ks < 16; ++ks) {
      const unsigned o = (unsigned)(((h * 8 + 2 * w + t) * 16 + ((4 * w + ks) & 15)) * 1024 + lane * 16);
      wq[t][ks] = ld16p(rs, (unsigned)OFF_WQKV + o);
      wk[t][ks] = ld16p(rs, (unsigned)(OFF_WQKV + 4 * MB) + o);
    }
  for (int i = 0; i < 32; ++i) {
    const int t = i >> 4, j = i & 15;
    *(LAS v4u*)(L + CH_WV + ((2 * w + t) * 16 + j) * 1024 + lane * 16) =
        ld16p(rs, (unsigned)(OFF_WQKV + 8 * MB) + (unsigned)(((h * 8 + 2 * w + t) * 16 + ((4 * w + j) & 15)) * 1024 + lane * 16));
  }
#pragma unroll
  for (int dt = 0; dt < 8; ++dt)
#pragma unroll
    for (int ks = 0; ks < 4; ++ks) wo[dt][ks] = ld16p(rs, (unsigned)OFF_WO + (unsigned)(((h * 32 + 8 * w + dt) * 4 + ks) * 1024 + lane * 16));
  v4u mo[4];
#pragma unroll
  for (int j = 0; j < 4; ++j) {
    mo[j] = *(const v4u*)(p.mem0 + r8 * DM + (4 * w + j) * 32 + g * 8);
    if (r < 8) *(LAS v4u*)(L + CH_MEM + (4 * w + j) * 512 + g * 128 + r * 16) = mo[j];
  }
  const unsigned qkv_base = (unsigned)(OFF_QKVF + (size_t)c * NSEG * NH * 24 * KB) + (unsigned)h * 24u * 1024u + 8192u;
  const unsigned kvm_base = (unsigned)(OFF_KVM + (size_t)c * NSEG * NH * 4 * KB) + (unsigned)h * 4096u;
  {
    wait_cnt<8>(rs, cnt_off(K_QKV, c, h), 1, 0, 1u, lane);
    v4u v[4];
#pragma unroll
    for (int i = 0; i < 4; ++i) v[i] = ld16(rs, qkv_base + (unsigned)((4 * w + i) * 1024 + lane * 16));
    SB();
#pragma unroll
    for (int i = 0; i < 4; ++i) *(LAS v4u*)(L + CH_KV + (4 * w + i) * 1024 + lane * 16) = v[i];
  }
  unsigned p_off[2], m_base;
  {
    const unsigned tab = wait_table(rs, c, lane);
#pragma unroll
    for (int q = 0; q < 2; ++q) {
      const unsigned ca = (unsigned)__builtin_amdgcn_readlane(tab, 4 * w + 2 * q) & 7u, cb = (unsigned)__builtin_amdgcn_readlane(tab, 4 * w + 2 * q + 1) & 7u;
      p_off[q] = (unsigned)((4 * w + 2 * q + (r >> 3)) * CR_BLK + g * 2 * ROW + h * 128 + r8 * 16) + (r < 8 ? ca : cb) * 4096u;
    }
    m_base = p_region(p.ws, c) + mem_copy(tab, xcc_id()) + (unsigned)((w >> 1) * ROW + (w & 1) * 2048 + g * 128 + r8 * 16);
  }
  unsigned have_qkv = 1;
  TICKDECL;
  volatile LAS v4u* FL = (volatile LAS v4u*)(L + CH_FL);
  if (tid == 0) *FL = v4u{0u, 0u, 0u, 0u};
  __syncthreads();
  v4u wv0[2], wv1[2];
#pragma unroll
  for (int j = 0; j < 2; ++j) {
    wv0[j] = *(const LAS v4u*)(L + CH_WV + 2 * w * 16384 + lane * 16 + j * 1024);
    wv1[j] = *(const LAS v4u*)(L + CH_WV + 2 * w * 16384 + 16384 + lane * 16 + j * 1024);
  }
  for (int s = 0; s < NSEG; ++s) {
    STAMP0(s, 0);
    const LAS unsigned char* MF = L + CH_MEM + g * 128 + r8 * 16;
    const LAS unsigned char* WV = L + CH_WV + 2 * w * 16384 + lane * 16;
    f32x4 aq0, aq1, ak0, ak1, av0, av1;
    v4u kx[8];
    {
      v4u fl = *FL;
      v4u mf[4];
      SB();
#define QKV_STEP(j, m) do { \
        MFA((j) == 0, aq0, wq[0][j], m); MFA((j) == 0, aq1, wq[1][j], m); SB(); \
        if ((j) >= 4 && (j) + 3 < 16) mf[((j) + 3) & 3] = *(const LAS v4u*)(MF + ((w + (((j) + 3) >> 2)) & 3) * 2048 + (((j) + 3) & 3) * 512); \
        MFA((j) == 0, ak0, wk[0][j], m); MFA((j) == 0, ak1, wk[1][j], m); SB(); \
        if ((j) >= 12) kx[(((j) - 12) & 1) + 4 * (((j) - 12) >> 1)] = *(const LAS v4u*)(L + CH_KV + ((((j) - 12) & 1) + 4 * (((j) - 12) >> 1)) * 1024 + lane * 16); \
        MFV((j) == 0, av0, m, wv0[(j) & 1]); SB(); \
        if ((j) + 2 < 16) wv0[(j) & 1] = *(const LAS v4u*)(WV + ((j) + 2) * 1024); \
        MFV((j) == 0, av1, m, wv1[(j) & 1]); SB(); \
        if ((j) + 2 < 16) wv1[(j) & 1] = *(const LAS v4u*)(WV + 16384 + ((j) + 2) * 1024); \
      } while (0)
      QKV_STEP(0, mo[0]);
      QKV_STEP(1, mo[1]);
      for (unsigned spins = 0; __builtin_amdgcn_readfirstlane(min(min(fl.x, fl.y), min(fl.z, fl.w))) < (unsigned)s && spins < (1u << 22); ++spins) fl = *FL;
#pragma unroll
      for (int i = 0; i < 3; ++i) mf[i] = *(const volatile LAS v4u*)(MF + ((w + 1) & 3) * 2048 + i * 512);
      SB();
      QKV_STEP(2, mo[2]);
      QKV_STEP(3, mo[3]);
#pragma unroll
      for (int j = 4; j < 16; ++j) { const v4u m = mf[j & 3]; QKV_STEP(j, m); }
#undef QKV_STEP
    }
    asm("s_nop 7\n\ts_nop 1" : "+v"(aq0), "+v"(aq1), "+v"(ak0), "+v"(ak1), "+v"(av0), "+v"(av1));
    {
      const v4u kq = r < 8 ? pack8(ak0, ak1) : pack8(aq0, aq1);
      *(LAS v4u*)(L + CH_EX + (r >> 3) * 4096 + w * 512 + g * 128 + r8 * 16) = kq;
      *(LAS v4u*)(L + CH_EX + 2048 + w * 512 + (g & 1) * 256 + r * 16) = pack8(av0, av1);
    }
    __syncthreads();
    STAMP0(s, 1);
    const LAS unsigned char* EXL = L + CH_EX + g * 128 + r8 * 16;
    f32x4 sx0, sx1, sm;
    {
      v4u qf[4], km[2];
#pragma unroll
      for (int i = 0; i < 4; ++i) qf[i] = *(const LAS v4u*)(EXL + 4096 + i * 512);
#pragma unroll
      for (int i = 0; i < 2; ++i) km[i] = *(const LAS v4u*)(EXL + i * 512);
#pragma unroll
      for (int i = 2; i < 4; ++i) kx[i] = *(const LAS v4u*)(L + CH_KV + i * 1024 + lane * 16);
      SB();
#pragma unroll
      for (int i = 0; i < 2; ++i) {
        MFV(i == 0, sx0, kx[i], qf[i]); MFV(i == 0, sx1, kx[4 + i], qf[i]);
        SB();
        kx[6 + i] = *(const LAS v4u*)(L + CH_KV + (6 + i) * 1024 + lane * 16);
        MFV(i == 0, sm, km[i], qf[i]);
        SB();
        km[i] = *(const LAS v4u*)(EXL + (2 + i) * 512);
      }
#pragma unroll
      for (int i = 2; i < 4; ++i) MFV(false, sx0, kx[i], qf[i]);
#pragma unroll
      for (int i = 2; i < 4; ++i) { MFV(false, sx1, kx[4 + i], qf[i]); MFV(false, sm, km[i - 2], qf[i]); }
      SB();
    }
    v4u vx[4], vma[4];
#pragma unroll
    for (int et = 0; et < 4; ++et) vx[et] = *(const LAS v4u*)(L + CH_KV + 8192 + et * 1024 + lane * 16);
#pragma unroll
    for (int m = 0; m < 4; ++m) vma[m] = *(const LAS v4u*)(L + CH_EX + 2048 + m * 512 + (g & 1) * 256 + r * 16);
    SB();
    mfma_settle3(sx0, sx1, sm);
    v4u p0, p1;
    {
      const bool lo = r < 8;
      const f32x4 sa = lo ? sx0 : sx1;
      unsigned u0 = pack2(sa.x, sa.y), u1 = pack2(sa.z, sa.w), u2 = pack2(lo ? sm.x : sm.z, lo ? sm.y : sm.w);
      u0 = pack2(bflo(u0) * kScale, bfhi(u0) * kScale);
      u1 = pack2(bflo(u1) * kScale, bfhi(u1) * kScale);
      u2 = pack2(bflo(u2) * kScale, bfhi(u2) * kScale);
      float a0 = bflo(u0), a1 = bfhi(u0), a2 = bflo(u1), a3 = bfhi(u1);
      float a4 = g < 2 ? bflo(u2) : kNegBig, a5 = g < 2 ? bfhi(u2) : kNegBig;
      float mx = fmaxf(fmaxf(fmaxf(a0, a1), fmaxf(a2, a3)), fmaxf(a4, a5));
      mx = fmaxf(mx, ror8f(mx));
      mx = xmax(mx);
      const float nm = -mx * kLog2e;
      a0 = __builtin_amdgcn_exp2f(fmaf(a0, kLog2e, nm)); a1 = __builtin_amdgcn_exp2f(fmaf(a1, kLog2e, nm));
      a2 = __builtin_amdgcn_exp2f(fmaf(a2, kLog2e, nm)); a3 = __builtin_amdgcn_exp2f(fmaf(a3, kLog2e, nm));
      a4 = __builtin_amdgcn_exp2f(fmaf(a4, kLog2e, nm)); a5 = __builtin_amdgcn_exp2f(fmaf(a5, kLog2e, nm));
      float sum = ((a0 + a1) + (a2 + a3)) + (a4 + a5);
      sum += ror8f(sum);
      sum = xsum(sum);
      const float inv = __builtin_amdgcn_rcpf(sum);
      u0 = pack2(a0 * inv, a1 * inv); u1 = pack2(a2 * inv, a3 * inv); u2 = pack2(a4 * inv, a5 * inv);
      const unsigned o0 = ror8(u0), o1 = ror8(u1), o2 = ror8(u2);
      p0.x = lo ? u0 : o0; p0.y = lo ? u1 : o1; p0.z = lo ? o0 : u0; p0.w = lo ? o1 : u1;
      p1.x = lo ? u2 : o2; p1.y = lo ? o2 : u2; p1.z = 0u; p1.w = 0u;
    }
    const v4u p1h = {0u, 0u, p1.x, p1.y};
    STAMP0(s, 6);
    if (s + 1 < NSEG) {
      v4u of[4];
      f32x4 o2[8], pa[8];
      unsigned pk[8][2];
      const unsigned soff = p_region(p.ws, c) + (unsigned)((s & 1) * ROW);
      mfma_pv(o2[0], o2[1], vx[0], vx[1], vma[0], p0, p1, p1h);
      SB();
      vx[0] = *(const LAS v4u*)(L + CH_KV + 8192 + 4 * 1024 + lane * 16);
      vx[1] = *(const LAS v4u*)(L + CH_KV + 8192 + 5 * 1024 + lane * 16);
      mfma_pv(o2[2], o2[3], vx[2], vx[3], vma[1], p0, p1, p1h);
      SB();
      vx[2] = *(const LAS v4u*)(L + CH_KV + 8192 + 6 * 1024 + lane * 16);
      vx[3] = *(const LAS v4u*)(L + CH_KV + 8192 + 7 * 1024 + lane * 16);
      mfma_pv(o2[4], o2[5], vx[0], vx[1], vma[2], p0, p1, p1h);
      SB();
      of[0] = pack8(o2[0], o2[1]);
      SB();
      mfma_pv(o2[6], o2[7], vx[2], vx[3], vma[3], p0, p1, p1h);
      SB();
      of[1] = pack8(o2[2], o2[3]);
      SB();
#pragma unroll
      for (int t = 0; t < 8; ++t) {
        MFV(true, pa[t], wo[t][0], of[0]);
        SB();
        if (t == 4) { of[2] = pack8(o2[4], o2[5]); SB(); }
      }
#pragma unroll
      for (int t = 0; t < 8; ++t) {
        MFV(false, pa[t], wo[t][1], of[1]);
        SB();
        if (t == 2) { of[3] = pack8(o2[6], o2[7]); SB(); }
      }
#define PACK_TILE(t) do { pk[t][0] = min(pack2(pa[t].x, pa[t].y), 0xfffffffeu); pk[t][1] = min(pack2(pa[t].z, pa[t].w), 0xfffffffeu); \
                          asm("" : "+v"(pk[t][0]), "+v"(pk[t][1])); SB(); } while (0)
#pragma unroll
      for (int t = 0; t < 8; ++t) {
        MFV(false, pa[t], wo[t][2], of[2]);
        MFV(false, pa[t], wo[t][3], of[3]);
        SB();
        if (t >= 4) PACK_TILE(t - 4);
      }
      {
        const v4u o = {r < 8 ? pk[0][0] : pk[2][0], r < 8 ? pk[0][1] : pk[2][1], r < 8 ? pk[1][0] : pk[3][0], r < 8 ? pk[1][1] : pk[3][1]};
        st16s(rs, p_off[0], soff, o);
        SB();
      }
      PACK_TILE(4);
      PACK_TILE(5);
      asm("s_nop 1" : "+v"(pa[6]), "+v"(pa[7]));
      PACK_TILE(6);
      PACK_TILE(7);
#undef PACK_TILE
      const v4u o = {r < 8 ? pk[4][0] : pk[6][0], r < 8 ? pk[4][1] : pk[6][1], r < 8 ? pk[5][0] : pk[7][0], r < 8 ? pk[5][1] : pk[7][1]};
      st16s(rs, p_off[1], soff, o);
      SB();
    }
    STAMP0(s, 2);
    st16(rs, kvm_base + (unsigned)(s * NH * 4096 + w * 1024 + lane * 16), *(const LAS v4u*)(L + CH_EX + w * 1024 + lane * 16));
    __syncthreads();
    const bool more = s + 1 < NSEG;
    const bool kv_known = more && have_qkv >= (unsigned)(s + 2);
    if (kv_known) {
      v4u v[4];
#pragma unroll
      for (int i = 0; i < 4; ++i) v[i] = ld16s(rs, (unsigned)(i * 1024 + lane * 16), qkv_base + (unsigned)((s + 1) * NH * 24 * 1024 + 4 * w * 1024));
      SB();
#pragma unroll
      for (int i = 0; i < 4; ++i) *(LAS v4u*)(L + CH_KV + (4 * w + i) * 1024 + lane * 16) = v[i];
    }
    DRAIN();
    __syncthreads();
    if (tid == 0) st4(rs, cnt_off(K_KM, c, h), (unsigned)(s + 1));
    STAMP0(s, 3);
    if (!more) { TICKDUMP; break; }
    {
      if (!kv_known) {
        unsigned spins = 0;
        for (;;) {
          have_qkv = __builtin_amdgcn_readfirstlane(ld4(rs, cnt_off(K_QKV, c, h)));
          if (have_qkv >= (unsigned)(s + 2)) break;
          if (spin_fail(spins, rs)) break;
          __builtin_amdgcn_s_sleep(4);
        }
        __builtin_amdgcn_fence(__ATOMIC_ACQUIRE, "wavefront");
        v4u v[4];
#pragma unroll
        for (int i = 0; i < 4; ++i) v[i] = ld16s(rs, (unsigned)(i * 1024 + lane * 16), qkv_base + (unsigned)((s + 1) * NH * 24 * 1024 + 4 * w * 1024));
        SB();
#pragma unroll
        for (int i = 0; i < 4; ++i) *(LAS v4u*)(L + CH_KV + (4 * w + i) * 1024 + lane * 16) = v[i];
      } else if (have_qkv < (unsigned)(s + 3)) {
        have_qkv = __builtin_amdgcn_readfirstlane(ld4(rs, cnt_off(K_QKV, c, h)));
        __builtin_amdgcn_fence(__ATOMIC_ACQUIRE, "wavefront");
      }
      STAMP0(s, 4);
#pragma unroll
      for (int j = 0; j < 2; ++j) {
        wv0[j] = *(const LAS v4u*)(L + CH_WV + 2 * w * 16384 + lane * 16 + j * 1024);
        wv1[j] = *(const LAS v4u*)(L + CH_WV + 2 * w * 16384 + 16384 + lane * 16 + j * 1024);
      }
      if (kv_known) __builtin_amdgcn_s_sleep(CH_SLEEP);
      const unsigned slot = (unsigned)(((s + 1) % 3) * M_SLOT);
      unsigned spins = 0;
      for (;;) {
#pragma unroll
        for (int j = 0; j < 4; ++j) mo[j] = ld16s(rs, m_base + (unsigned)(j * 512), slot);
        SB();
        unsigned mx = 0;
#pragma unroll
        for (int j = 0; j < 4; ++j) mx = max(mx, max(max(mo[j].x, mo[j].y), max(mo[j].z, mo[j].w)));
        if (__all(mx != 0xffffffffu)) break;
        if (spin_fail(spins, rs)) break;
      }
      STAMP0(s, 5);
      if (r < 8) {
#pragma unroll
        for (int j = 0; j < 4; ++j) *(LAS v4u*)(L + CH_MEM + (4 * w + j) * 512 + g * 128 + r * 16) = mo[j];
      }
      LDSWAIT();
      if (lane == 0) ((volatile LAS unsigned*)FL)[w] = (unsigned)(s + 1);
      asm volatile("" ::: "memory");
    }
  }
}

__device__ __forceinline__ void role_creduce(const Params& p, rsrc_t rs, LAS unsigned char* L, int c, int idx) {
  const int tid = threadIdx.x, lane = tid & 63, w = __builtin_amdgcn_readfirstlane(tid >> 6);
  const unsigned l16 = (unsigned)lane * 16u;
  const unsigned xcc = xcc_id();
  const unsigned blk = p_region(p.ws, c) + (unsigned)(idx * CR_BLK);
  unsigned col;
  {
    volatile LAS unsigned* T = (volatile LAS unsigned*)L;
    if (w == 0) {
      for (int i = 0; i < CAL_WAIT; ++i) __builtin_amdgcn_s_sleep(127);
      unsigned best = 0xffffffffu, arg = 0;
#pragma unroll 1
      for (int q = 0; q < 8 * CAL_N; ++q) {
        const unsigned t0 = (unsigned)__builtin_amdgcn_s_memtime();
        unsigned y = ld4(rs, blk + (unsigned)((q & 7) * 4096 + (q >> 3) * 1024));
        asm volatile("s_waitcnt vmcnt(0)" : "+v"(y) :: "memory");
        const unsigned t1 = (unsigned)__builtin_amdgcn_s_memtime();
        if (t1 - t0 < best) { best = t1 - t0; arg = (unsigned)q & 7u; }
      }
      if (lane == 0) T[0] = arg;
    }
    __syncthreads();
    col = __builtin_amdgcn_readfirstlane(T[0]);
    const v4u o = {~0u, ~0u, ~0u, ~0u};
#pragma unroll
    for (int k = 0; k < 8; ++k) st16s(rs, l16 + (unsigned)(k & 3) * 1024u, blk + (unsigned)((2 * w + (k >> 2)) * ROW) + col * 4096u, o);
#pragma unroll
    for (int k = 0; k < 6; ++k) st16s(rs, l16 + (unsigned)w * 1024u, blk + (unsigned)(M_ROW0 + k * ROW) + col * 4096u, o);
    DRAIN();
    __syncthreads();
    if (tid == 0) st4(rs, cnt_off(K_COL, c, idx), 1u + col + 8u * xcc);
  }
  const unsigned p_base = blk + (unsigned)(2 * w * ROW) + col * 4096u;
  v4u sel;
  {
    const unsigned one = (lane & 1) ? 0x3f800000u : 0x00003f80u, q = ((unsigned)lane & 7u) >> 1;
    sel.x = q == 0u ? one : 0u; sel.y = q == 1u ? one : 0u; sel.z = q == 2u ? one : 0u; sel.w = q == 3u ? one : 0u;
  }
  unsigned m_off0, m_off1;
  {
    const unsigned tab = wait_table(rs, c, lane);
    const unsigned x0 = 2u * ((unsigned)lane >> 5) + (((unsigned)lane >> 3) & 1u);
    unsigned c0 = 0, c1 = 0;
#pragma unroll
    for (unsigned x = 0; x < 4; ++x) {
      const unsigned a = mem_copy(tab, x), b = mem_copy(tab, x + 4u);
      c0 = x0 == x ? a : c0; c1 = x0 == x ? b : c1;
    }
    const unsigned m_in = p_region(p.ws, c) + (unsigned)((idx >> 3) * ROW + (idx & 7) * 512 + w * 128 + (lane & 7) * 16 + ((lane >> 4) & 1) * 8);
    m_off0 = m_in + c0;
    m_off1 = m_in + c1;
  }
  for (int s = 0; s + 1 < NSEG; ++s) {
    const unsigned pb = p_base + (unsigned)((s & 1) * ROW);
    v4u x[4]; unsigned spins = 0;
    if (s > 0) __builtin_amdgcn_s_sleep(CR_SLEEP);
    for (;;) {
#pragma unroll
      for (int k = 0; k < 4; ++k) x[k] = ld16s(rs, l16 + (unsigned)k * 1024u, pb);
      SB();
      unsigned mx = 0;
#pragma unroll
      for (int k = 0; k < 4; ++k) mx = max(mx, max(max(x[k].x, x[k].y), max(x[k].z, x[k].w)));
      if (__all(mx != 0xffffffffu)) break;
      if (spin_fail(spins, rs)) break;
    }
    STAMP(s, w);
    f32x4 a, a2;
    mfma_k2(a, sel, x[0], x[1]);
    mfma_k2(a2, sel, x[2], x[3]);
    mfma_settle2(a, a2);
    a += a2;
    a.x += ror8f(a.x); a.y += ror8f(a.y); a.z += ror8f(a.z); a.w += ror8f(a.w);
    v2u o; o.x = min(pack2(a.x, a.y), 0xfffffffeu); o.y = min(pack2(a.z, a.w), 0xfffffffeu);
    const unsigned slot = (unsigned)(((s + 1) % 3) * M_SLOT);
    st8(rs, m_off0 + slot, o);
    st8(rs, m_off1 + slot, o);
    __builtin_amdgcn_s_sleep(16);
    const v4u ones4 = {~0u, ~0u, ~0u, ~0u};
#pragma unroll
    for (int k = 0; k < 4; ++k) st16s(rs, l16 + (unsigned)k * 1024u, pb, ones4);
    const unsigned old = (unsigned)((s % 3) * M_SLOT);
    const v2u ones = {~0u, ~0u};
    st8(rs, m_off0 + old, ones);
    st8(rs, m_off1 + old, ones);
  }
}

__device__ __forceinline__ void role_qkv(rsrc_t rs, LAS unsigned char* L, int c, int h) {
  const int tid = threadIdx.x, lane = tid & 63, w = __builtin_amdgcn_readfirstlane(tid >> 6);
  const int g = lane >> 4, r = lane & 15;
  v4u wq[2][16], wk[2][16], wv[2][16];
#pragma unroll
  for (int t = 0; t < 2; ++t)
#pragma unroll
    for (int ks = 0; ks < 16; ++ks) {
      const unsigned o = (unsigned)(((h * 8 + 2 * w + t) * 16 + ks) * 1024 + lane * 16);
      wq[t][ks] = ld16p(rs, (unsigned)OFF_WQKV + o);
      wk[t][ks] = ld16p(rs, (unsigned)(OFF_WQKV + 4 * MB) + o);
      wv[t][ks] = ld16p(rs, (unsigned)(OFF_WQKV + 8 * MB) + o);
    }
  const int fb = r * RSTR + g * 16;
  volatile LAS unsigned* RDY = (volatile LAS unsigned*)(L + 2 * IMG);
  const unsigned xbase = (unsigned)(OFF_XA + (size_t)c * 2 * MB);
  bool staged = false;
  TICKDECL;
  for (int s = 0; s < NSEG; ++s) {
    LAS unsigned char* X = L + (s & 1) * IMG;
    if (!staged) {
      if (c == 1) wait_cnt<POLL>(rs, cnt_off(K_X2, 0, 8 * w), 8, h & 7, (unsigned)(s + 1), lane);
      STAMP0(s, 0);
      v4u v[8];
      rows_load(rs, xbase + (unsigned)s * 32768u, v, w, lane);
      rows_put(X, v, w, lane);
      __syncthreads();
    }
    STAMP0(s, 1);
    unsigned nx = 0xffffffffu;
    if (c == 1 && w == 0) nx = ld4(rs, cnt_off(K_X2, 0, 0) + (unsigned)(lane & 31) * 128u);
    v4u nv[8];
    if (c == 0 && s + 1 < NSEG) rows_load(rs, xbase + (unsigned)(s + 1) * 32768u, nv, w, lane);
    f32x4 aq[2][2], ak[2][2], av[2][2];
    {
      v4u xf[4];
#pragma unroll
      for (int i = 0; i < 4; ++i) xf[i] = row_frag(X, i & 1, i >> 1, fb);
      SB();
#pragma unroll
      for (int i = 0; i < 32; ++i) {
        const int ks = i >> 1, tt = i & 1;
        const v4u x = xf[i & 3];
        MFA(ks == 0, aq[0][tt], wq[0][ks], x);
        MFA(ks == 0, aq[1][tt], wq[1][ks], x);
        MFA(ks == 0, ak[0][tt], wk[0][ks], x);
        SB();
        if (i >= 1 && i + 3 < 32) xf[(i + 3) & 3] = row_frag(X, (i + 3) & 1, (i + 3) >> 1, fb);
        MFA(ks == 0, ak[1][tt], wk[1][ks], x);
        MFV(ks == 0, av[0][tt], x, wv[0][ks]);
        MFV(ks == 0, av[1][tt], x, wv[1][ks]);
        SB();
      }
    }
    mfma_settle(av[0][0], av[1][0], av[0][1], av[1][1]);
    mfma_settle(aq[0][0], aq[1][0], ak[0][0], ak[1][0]);
    mfma_settle(aq[0][1], aq[1][1], ak[0][1], ak[1][1]);
    STAMP0(s, 2);
    const unsigned ob = (unsigned)(OFF_QKVF + (size_t)((c * NSEG + s) * NH + h) * 24 * KB) + (unsigned)lane * 16u;
#pragma unroll
    for (int tt = 0; tt < 2; ++tt) {
      st16(rs, ob + (unsigned)(8192 + (tt * 4 + w) * 1024), pack8(ak[0][tt], ak[1][tt]));
      st16(rs, ob + (unsigned)((tt * 4 + w) * 1024), pack8(aq[0][tt], aq[1][tt]));
    }
#pragma unroll
    for (int t = 0; t < 2; ++t) st16(rs, ob + (unsigned)(16384 + (2 * w + t) * 1024), pack8(av[t][0], av[t][1]));
    if (c == 0) {
      if (s + 1 < NSEG) rows_put(L + ((s + 1) & 1) * IMG, nv, w, lane);
      staged = true;
    } else if (w == 0) {
      const bool ok = __all(nx >= (unsigned)(s + 2)) && s + 1 < NSEG;
      if (lane == 0) RDY[0] = ok ? 1u : 0u;
    }
    DRAIN();
    __syncthreads();
    if (tid == 0) st4(rs, cnt_off(K_QKV, c, h), (unsigned)(s + 1));
    STAMP0(s, 3);
    if (c == 1) {
      staged = RDY[0] != 0u;
      if (staged) {
        v4u v[8];
        rows_load(rs, xbase + (unsigned)(s + 1) * 32768u, v, w, lane);
        rows_put(L + ((s + 1) & 1) * IMG, v, w, lane);
        __syncthreads();
      }
    }
  }
}

__device__ __forceinline__ void role_att(rsrc_t rs, LAS unsigned char* L, int c, int pp) {
  const int tid = threadIdx.x, lane = tid & 63, w = __builtin_amdgcn_readfirstlane(tid >> 6);
  const int g = lane >> 4, r = lane & 15, r8 = lane & 7;
  v4u wo[2][8][4];
#pragma unroll
  for (int hh = 0; hh < 2; ++hh)
#pragma unroll
    for (int dt = 0; dt < 8; ++dt)
#pragma unroll
      for (int ks = 0; ks < 4; ++ks)
        wo[hh][dt][ks] = ld16p(rs, (unsigned)OFF_WO + (unsigned)((((2 * pp + hh) * 32 + 8 * w + dt) * 4 + ks) * 1024 + lane * 16));
  const int hh = w >> 1, tt = w & 1, h = 2 * pp + hh;
  const int q = 16 * tt + r;
  unsigned ok0 = 0, ok1 = 0;
#pragma unroll
  for (int j = 0; j < 4; ++j) { ok0 |= (4 * g + j <= q ? 1u : 0u) << j; ok1 |= (16 + 4 * g + j <= q ? 1u : 0u) << j; }
  LAS unsigned char* EX = L;
  LAS unsigned char* TR = L + 16384;
  const unsigned qbase = (unsigned)(OFF_QKVF + (size_t)(c * NSEG * NH + h) * 24 * KB);
  const unsigned mbase = (unsigned)(OFF_KVM + (size_t)(c * NSEG * NH + h) * 4 * KB);
  const unsigned l16 = (unsigned)lane * 16u;
  unsigned have_qkv = 0, have_km = 0;
  v4u qf[4], kf[8], vf[8];
  bool loaded = false;
  TICKDECL;
  for (int s = 0; s < NSEG; ++s) {
    if (!loaded) {
      if (have_qkv < (unsigned)(s + 1)) wait_cnt<8>(rs, cnt_off(K_QKV, c, h), 1, 0, (unsigned)(s + 1), lane);
      const unsigned ib = qbase + (unsigned)(s * NH * 24 * 1024);
#pragma unroll
      for (int ks = 0; ks < 4; ++ks) qf[ks] = ld16s(rs, l16 + (unsigned)(ks * 1024), ib + (unsigned)(tt * 4096));
#pragma unroll
      for (int i = 0; i < 8; ++i) kf[i] = ld16s(rs, l16 + (unsigned)((i & 3) * 1024), ib + (unsigned)(8192 + (i & 4) * 1024));
#pragma unroll
      for (int i = 0; i < 8; ++i) vf[i] = ld16s(rs, l16 + (unsigned)((i & 3) * 1024), ib + (unsigned)(16384 + (i & 4) * 1024));
      SB();
    }
    f32x4 sx0, sx1, sm;
#pragma unroll
    for (int ks = 0; ks < 4; ++ks) { MFV(ks == 0, sx0, kf[ks], qf[ks]); MFV(ks == 0, sx1, kf[4 + ks], qf[ks]); }
    SB();
    if (have_km < (unsigned)(s + 1)) wait_cnt<POLL>(rs, cnt_off(K_KM, c, h), 1, 0, (unsigned)(s + 1), lane);
    STAMP0(s, 0);
    const unsigned mb = mbase + (unsigned)(s * NH * 4096);
    v4u km[4], vma[4];
#pragma unroll
    for (int ks = 0; ks < 4; ++ks) km[ks] = ld16s(rs, (unsigned)(ks * 512 + g * 128 + r8 * 16), mb);
#pragma unroll
    for (int m = 0; m < 4; ++m) vma[m] = ld16s(rs, (unsigned)(2048 + m * 512 + (g & 1) * 256 + r * 16), mb);
    const unsigned nq = ld4(rs, cnt_off(K_QKV, c, h));
    SB();
#pragma unroll
    for (int ks = 0; ks < 4; ++ks) MFV(ks == 0, sm, km[ks], qf[ks]);
    mfma_settle3(sx0, sx1, sm);
    STAMP0(s, 4);
    v4u p0, p1;
    softmax_frags(sx0, sx1, sm, ok0, ok1, g < 2, p0, p1);
    const v4u p1h = {0u, 0u, p1.x, p1.y};
    STAMP0(s, 5);
    {
      f32x4 o2[8];
#pragma unroll
      for (int m = 0; m < 4; ++m)
        mfma_pv(o2[2 * m], o2[2 * m + 1], vf[2 * m], vf[2 * m + 1], vma[m], p0, p1, p1h);
      mfma_settle(o2[0], o2[1], o2[2], o2[3]);
      mfma_settle(o2[4], o2[5], o2[6], o2[7]);
#pragma unroll
      for (int m = 0; m < 4; ++m) *(LAS v4u*)(EX + (w * 4 + m) * 1024 + lane * 16) = pack8(o2[2 * m], o2[2 * m + 1]);
    }
    have_qkv = __builtin_amdgcn_readfirstlane(nq);
    loaded = s + 1 < NSEG && have_qkv >= (unsigned)(s + 2);
    STAMP0(s, 6);
    __syncthreads();
    STAMP0(s, 7);
    f32x4 acc[8][2];
    const unsigned pout = (unsigned)(OFF_PO + (size_t)(s * NATT + pp) * 32 * KB);
    {
      const unsigned ib = qbase + (unsigned)((loaded ? s + 1 : s) * NH * 24 * 1024);
      v4u of[4], rw;
#define OFRAG(i) ((((i) >> 2) & 1) * 8 + ((i) >> 3) * 4 + ((i) & 3))
#pragma unroll
      for (int i = 0; i < 4; ++i) of[i] = *(const LAS v4u*)(EX + OFRAG(i) * 1024 + lane * 16);
      SB();
#pragma unroll
      for (int i = 0; i < 16; ++i) {
        const int t2 = i >> 3, h2 = (i >> 2) & 1, ks = i & 3;
        const bool first = h2 == 0 && ks == 0;
        const v4u x = of[i & 3];
#pragma unroll
        for (int dt = 0; dt < 4; ++dt) MFA(first, acc[dt][t2], wo[h2][dt][ks], x);
        SB();
        if (i >= 1 && i + 3 < 16) of[(i + 3) & 3] = *(const LAS v4u*)(EX + OFRAG(i + 3) * 1024 + lane * 16);
        if (i == 9) partial_put(TR, acc, 0, w, lane);
        if (i >= 11 && i < 15) rw = partial_row(TR, 0, i - 11, w, lane);
        MFA(first, acc[4][t2], wo[h2][4][ks], x);
        MFA(first, acc[5][t2], wo[h2][5][ks], x);
        SB();
        if (i < 4) qf[i] = ld16s(rs, l16 + (unsigned)(i * 1024), ib + (unsigned)(tt * 4096));
        else if (i < 12) kf[i - 4] = ld16s(rs, l16 + (unsigned)(((i - 4) & 3) * 1024), ib + (unsigned)(8192 + ((i - 4) & 4) * 1024));
        else vf[i - 12] = ld16s(rs, l16 + (unsigned)((i - 12) * 1024), ib + 16384u);
        MFA(first, acc[6][t2], wo[h2][6][ks], x);
        SB();
        if (i >= 12) vf[i - 8] = ld16s(rs, l16 + (unsigned)((i - 12) * 1024), ib + 20480u);
        if (i >= 11 && i < 15) partial_row_store(rs, pout, rw, 0, i - 11, w, lane);
        MFA(first, acc[7][t2], wo[h2][7][ks], x);
        SB();
        if (i == 10) __syncthreads();
      }
#undef OFRAG
    }
    mfma_settle(acc[0][1], acc[1][1], acc[2][1], acc[3][1]);
    mfma_settle(acc[4][1], acc[5][1], acc[6][1], acc[7][1]);
    STAMP0(s, 2);
    partial_put(TR, acc, 1, w, lane);
    __syncthreads();
    partial_store(rs, pout, TR, 1, w, lane);
    const unsigned nk = ld4(rs, cnt_off(K_KM, c, h));
    DRAIN();
    have_km = __builtin_amdgcn_readfirstlane(nk);
    __syncthreads();
    if (tid == 0) st4(rs, cnt_off(K_ATT, c, pp), (unsigned)(s + 1));
    STAMP0(s, 3);
  }
  TICKDUMP;
}

__device__ __forceinline__ float gelu_tanh(float x) {
  const float z = x * fmaf(x * x, -2.0f * 0.7978845608028654f * 0.044715f * 1.4426950408889634f, -2.0f * 0.7978845608028654f * 1.4426950408889634f);
  return x * __builtin_amdgcn_rcpf(1.0f + __builtin_amdgcn_exp2f(z));
}
__device__ __forceinline__ unsigned geglu2(float g0, float g1, float u0, float u1) {
  const unsigned gp = pack2(g0, g1), up = pack2(u0, u1);
  const unsigned ap = pack2(gelu_tanh(bflo(gp)), gelu_tanh(bfhi(gp)));
  return pack2(bflo(ap) * bflo(up), bfhi(ap) * bfhi(up));
}
__device__ __forceinline__ void role_ffn(rsrc_t rs, LAS unsigned char* L, int c, int sl) {
  const int tid = threadIdx.x, lane = tid & 63, w = __builtin_amdgcn_readfirstlane(tid >> 6);
  const int g = lane >> 4, r = lane & 15;
  v4u wg[2][16], wu[2][16], wd[8][4];
#pragma unroll
  for (int t = 0; t < 2; ++t)
#pragma unroll
    for (int ks = 0; ks < 16; ++ks) {
      const unsigned o = (unsigned)(((sl * 8 + 2 * w + t) * 16 + ks) * 1024 + lane * 16);
      wg[t][ks] = ld16p(rs, (unsigned)OFF_WG + o);
      wu[t][ks] = ld16p(rs, (unsigned)OFF_WU + o);
    }
#pragma unroll
  for (int dt = 0; dt < 8; ++dt)
#pragma unroll
    for (int ks = 0; ks < 4; ++ks) wd[dt][ks] = ld16p(rs, (unsigned)OFF_WD + (unsigned)(((sl * 32 + 8 * w + dt) * 4 + ks) * 1024 + lane * 16));
  LAS unsigned char* X = L;
  LAS unsigned char* EX = L + IMG;
  LAS unsigned char* TR = L + IMG + 8192;
  const int fb = r * RSTR + g * 16;
  volatile LAS unsigned* RDY = (volatile LAS unsigned*)(L + 2 * IMG + 8192);
  const unsigned xbase = (unsigned)(OFF_XF + (size_t)c * 2 * MB);
  bool staged = false;
  TICKDECL;
  for (int s = 0; s < NSEG; ++s) {
    if (!staged) {
      wait_cnt<POLL>(rs, cnt_off(K_X1, c, 8 * w), 8, sl & 7, (unsigned)(s + 1), lane);
      STAMP0(s, 0);
      v4u v[8];
      rows_load(rs, xbase + (unsigned)s * 32768u, v, w, lane);
      rows_put(X, v, w, lane);
      __syncthreads();
    }
    STAMP0(s, 1);
    f32x4 ag[2][2], au[2][2];
    {
      v4u xf[4];
#pragma unroll
      for (int i = 0; i < 4; ++i) xf[i] = row_frag(X, 0, i, fb);
      SB();
      unsigned hv[4];
#pragma unroll
      for (int i = 0; i < 32; ++i) {
        const int ks = i & 15, tt = i >> 4;
        const v4u x = xf[i & 3];
        MFA(ks == 0, ag[0][tt], wg[0][ks], x);
        MFA(ks == 0, ag[1][tt], wg[1][ks], x);
        SB();
        if (i >= 1 && i + 3 < 32) xf[(i + 3) & 3] = row_frag(X, (i + 3) >> 4, (i + 3) & 15, fb);
        MFA(ks == 0, au[0][tt], wu[0][ks], x);
        MFA(ks == 0, au[1][tt], wu[1][ks], x);
        SB();
        if (i >= 19 && i < 27 && (i & 1)) {
          const int q = (i - 19) >> 1, t = q >> 1, j = (q & 1) * 2;
          hv[q] = geglu2(ag[t][0][j], ag[t][0][j + 1], au[t][0][j], au[t][0][j + 1]);
          SB();
        }
        if (i == 27) { *(LAS v4u*)(EX + w * 1024 + lane * 16) = v4u{hv[0], hv[1], hv[2], hv[3]}; SB(); }
      }
    }
    STAMP0(s, 4);
    __syncthreads();
    STAMP0(s, 5);
    const unsigned pout = (unsigned)(OFF_PF + (size_t)(s * NFFN + sl) * 32 * KB);
    f32x4 acc[8][2];
    unsigned nx = 0;
    {
      v4u hf[4];
#pragma unroll
      for (int i = 0; i < 4; ++i) hf[i] = *(const LAS v4u*)(EX + i * 1024 + lane * 16);
      mfma_settle(ag[0][1], ag[1][1], au[0][1], au[1][1]);
      SB();
      unsigned hv[4];
#pragma unroll
      for (int ks = 0; ks < 4; ++ks) {
        const int t = ks >> 1, j = (ks & 1) * 2;
        const unsigned gp = pack2(ag[t][1][j], ag[t][1][j + 1]), up = pack2(au[t][1][j], au[t][1][j + 1]);
        MFV(ks == 0, acc[0][0], wd[0][ks], hf[ks]); MFV(ks == 0, acc[1][0], wd[1][ks], hf[ks]);
        SB();
        const float g0 = gelu_tanh(bflo(gp));
        MFV(ks == 0, acc[2][0], wd[2][ks], hf[ks]); MFV(ks == 0, acc[3][0], wd[3][ks], hf[ks]);
        SB();
        const float g1 = gelu_tanh(bfhi(gp));
        MFV(ks == 0, acc[4][0], wd[4][ks], hf[ks]); MFV(ks == 0, acc[5][0], wd[5][ks], hf[ks]);
        SB();
        const unsigned ap = pack2(g0, g1);
        hv[ks] = pack2(bflo(ap) * bflo(up), bfhi(ap) * bfhi(up));
        MFV(ks == 0, acc[6][0], wd[6][ks], hf[ks]); MFV(ks == 0, acc[7][0], wd[7][ks], hf[ks]);
        SB();
      }
      *(LAS v4u*)(EX + (4 + w) * 1024 + lane * 16) = v4u{hv[0], hv[1], hv[2], hv[3]};
      mfma_settle(acc[0][0], acc[1][0], acc[2][0], acc[3][0]);
      mfma_settle(acc[4][0], acc[5][0], acc[6][0], acc[7][0]);
      partial_put(TR, acc, 0, w, lane);
      if (w == 0) nx = ld4(rs, cnt_off(K_X1, c, 0) + (unsigned)(lane & 31) * 128u);
      STAMP0(s, 6);
      __syncthreads();
      STAMP0(s, 7);
#pragma unroll
      for (int i = 0; i < 4; ++i) hf[i] = *(const LAS v4u*)(EX + (4 + i) * 1024 + lane * 16);
      v4u rw = partial_row(TR, 0, 0, w, lane);
      SB();
#pragma unroll
      for (int ks = 0; ks < 4; ++ks) {
#pragma unroll
        for (int dt = 0; dt < 4; ++dt) MFV(ks == 0, acc[dt][1], wd[dt][ks], hf[ks]);
        SB();
        partial_row_store(rs, pout, rw, 0, ks, w, lane);
        MFV(ks == 0, acc[4][1], wd[4][ks], hf[ks]); MFV(ks == 0, acc[5][1], wd[5][ks], hf[ks]);
        SB();
        if (ks < 3) rw = partial_row(TR, 0, ks + 1, w, lane);
        MFV(ks == 0, acc[6][1], wd[6][ks], hf[ks]); MFV(ks == 0, acc[7][1], wd[7][ks], hf[ks]);
        SB();
      }
    }
    mfma_settle(acc[0][1], acc[1][1], acc[2][1], acc[3][1]);
    mfma_settle(acc[4][1], acc[5][1], acc[6][1], acc[7][1]);
    STAMP0(s, 2);
    partial_put(TR, acc, 1, w, lane);
    if (w == 0) {
      const bool ok = __all(nx >= (unsigned)(s + 2)) && s + 1 < NSEG;
      if (lane == 0) RDY[0] = ok ? 1u : 0u;
    }
    __syncthreads();
    staged = RDY[0] != 0u;
    if (staged) {
      v4u nv[8];
      partial_store(rs, pout, TR, 1, w, lane);
      rows_load(rs, xbase + (unsigned)(s + 1) * 32768u, nv, w, lane);
      rows_put(X, nv, w, lane);
      DRAIN();
      __syncthreads();
      if (tid == 0) st4(rs, cnt_off(K_FFN, c, sl), (unsigned)(s + 1));
    } else {
      partial_store(rs, pout, TR, 1, w, lane);
      DRAIN();
      __syncthreads();
      if (tid == 0) st4(rs, cnt_off(K_FFN, c, sl), (unsigned)(s + 1));
    }
    STAMP0(s, 3);
  }
  TICKDUMP;
}

template <int NP>
__device__ __forceinline__ void sum_rows(rsrc_t rs, unsigned off, float (&a)[8]) {
  v4u v[NP];
#pragma unroll
  for (int i = 0; i < NP; ++i) v[i] = ld16(rs, off + (unsigned)i * 32768u);
  SB();
#pragma unroll
  for (int j = 0; j < 8; ++j) a[j] = 0.f;
  unsigned c0 = 0x00003f80u, c1 = 0x3f800000u;
  asm("" : "+v"(c0), "+v"(c1));
  const bf16x2 e0 = __builtin_bit_cast(bf16x2, c0), e1 = __builtin_bit_cast(bf16x2, c1);
#pragma unroll
  for (int i = 0; i < NP; ++i) {
#pragma unroll
    for (int q = 0; q < 4; ++q) {
      const unsigned xu = v[i][q];
      const bf16x2 x = __builtin_bit_cast(bf16x2, xu);
      a[2 * q] = __builtin_amdgcn_fdot2_f32_bf16(x, e0, a[2 * q], false);
      a[2 * q + 1] = __builtin_amdgcn_fdot2_f32_bf16(x, e1, a[2 * q + 1], false);
    }
  }
}
__device__ __forceinline__ void role_red(const Params& p, rsrc_t rs, LAS unsigned char* L, int t) {
  const int tid = threadIdx.x, lane = tid & 63, w = __builtin_amdgcn_readfirstlane(tid >> 6);
  const int c = w >> 1, ffn = w & 1;
  for (int s = 0; s < NSEG; ++s) {
    float a[8];
    if (!ffn) {
      wait_cnt<POLL>(rs, cnt_off(K_ATT, c, 0), NATT, t & 15, (unsigned)(s + 1), lane);
      STAMP(s, 2 * w);
      sum_rows<NATT>(rs, (unsigned)(OFF_PO + (size_t)s * NATT * 32 * KB) + (unsigned)(t * 1024 + lane * 16), a);
    } else {
      wait_cnt<POLL>(rs, cnt_off(K_FFN, c, 0), NFFN, t % NFFN, (unsigned)(s + 1), lane);
      STAMP(s, 2 * w);
      sum_rows<NFFN>(rs, (unsigned)(OFF_PF + (size_t)s * NFFN * 32 * KB) + (unsigned)(t * 1024 + lane * 16), a);
    }
    v4u xo;
    if (w == 0) xo = *(const v4u*)(p.x + (size_t)(s * SEG + t) * DM + lane * 8);
    else xo = *(volatile LAS v4u*)(L + s * 1024 + lane * 16);
    float x[8] = {bflo(xo.x), bfhi(xo.x), bflo(xo.y), bfhi(xo.y), bflo(xo.z), bfhi(xo.z), bflo(xo.w), bfhi(xo.w)};
#pragma unroll
    for (int j = 0; j < 8; ++j) x[j] = bf16r(x[j] + bf16r(a[j]));
    v4u xn; xn.x = pack2(x[0], x[1]); xn.y = pack2(x[2], x[3]); xn.z = pack2(x[4], x[5]); xn.w = pack2(x[6], x[7]);
    if (w == 3) {
      *(v4u*)(p.out + (size_t)(s * SEG + t) * DM + lane * 8) = xn;
    } else {
      *(volatile LAS v4u*)(L + s * 1024 + lane * 16) = xn;
      float y[8];
      layernorm8(x, y);
      v4u o; o.x = pack2(y[0], y[1]); o.y = pack2(y[2], y[3]); o.z = pack2(y[4], y[5]); o.w = pack2(y[6], y[7]);
      const unsigned dst = (ffn ? (unsigned)(OFF_XA + 2 * MB) : (unsigned)(OFF_XF + (size_t)c * 2 * MB)) + (unsigned)((s * SEG + t) * 1024 + lane * 16);
      st16(rs, dst, o);
      LDSWAIT();
      DRAIN();
      const unsigned f = ffn ? cnt_off(K_X2, 0, t) : cnt_off(K_X1, c, t);
      if (lane == 0) st4(rs, f, (unsigned)(s + 1));
    }
    STAMP(s, 2 * w + 1);
  }
}

__global__ void __launch_bounds__(256, 1) k_main(Params p) {
  __shared__ __attribute__((aligned(16))) unsigned char lds[LDS_BYTES];
  LAS unsigned char* L = (LAS unsigned char*)lds;
  const rsrc_t rs = __builtin_amdgcn_make_buffer_rsrc((void*)p.ws, 0, (int)WS_BYTES, 0x00020000);
  const int b = blockIdx.x;
  if (b < 64) role_chain(p, rs, L, b >> 5, b & 31);
  else if (b < 96) role_creduce(p, rs, L, (b - 64) >> 4, (b - 64) & 15);
  else if (b < 160) role_qkv(rs, L, (b - 96) >> 5, (b - 96) & 31);
  else if (b < 192) role_att(rs, L, (b - 160) >> 4, (b - 160) & 15);
  else if (b < 220) role_ffn(rs, L, (b - 192) / NFFN, (b - 192) % NFFN);
  else if (b < 252) role_red(p, rs, L, b - 220);
}

extern "C" void kernel_launch(void* const* d_in, const int* in_sizes, int n_in,
                              void* d_out, int out_size, void* d_ws, size_t ws_size,
                              hipStream_t stream) {
  PrepParams pp;
  pp.x = (const u16*)d_in[0];
  pp.wq = (const u16*)d_in[2];
  pp.wk = (const u16*)d_in[3];
  pp.wv = (const u16*)d_in[4];
  pp.wo = (const u16*)d_in[5];
  pp.wg = (const u16*)d_in[8];
  pp.wu = (const u16*)d_in[9];
  pp.wd = (const u16*)d_in[10];
  pp.ws = (unsigned char*)d_ws;
  k_prep<<<NJ_ALL, 256, 0, stream>>>(pp);
  Params p;
  p.x = (const u16*)d_in[0];
  p.mem0 = (const u16*)d_in[1];
  p.out = (u16*)d_out;
  p.ws = (unsigned char*)d_ws;
  k_main<<<252, 256, 0, stream>>>(p);
}
```

```cpp
#include <hip/hip_runtime.h>
#include <hip/hip_bf16.h>
#include <stdint.h>

#define GAS __attribute__((address_space(1)))
#define LAS __attribute__((address_space(3)))
typedef unsigned v4u __attribute__((ext_vector_type(4)));
typedef unsigned v2u __attribute__((ext_vector_type(2)));
typedef float f32x4 __attribute__((ext_vector_type(4)));
typedef float f32x2 __attribute__((ext_vector_type(2)));
typedef short bf16x8 __attribute__((ext_vector_type(8)));
typedef __bf16 bf16x2 __attribute__((ext_vector_type(2)));
typedef GAS unsigned gu32;
typedef unsigned short u16;
#define RLX_AGENT __ATOMIC_RELAXED, __HIP_MEMORY_SCOPE_AGENT
#define SB() __builtin_amdgcn_sched_barrier(0)
#define DRAIN() asm volatile("s_waitcnt vmcnt(0)" ::: "memory")
#define LDSWAIT() asm volatile("s_waitcnt lgkmcnt(0)" ::: "memory")

constexpr int S_TOK = 2048, DM = 512, NH = 32, DH = 128, NMEM = 8, SEG = 32, NSEG = 64, DFF = 1792;
constexpr int NFFN = 14, NATT = 16;

constexpr size_t KB = 1024, MB = 1024 * 1024;
constexpr size_t OFF_ABORT = 0;
enum { K_QKV = 0, K_KM = 1, K_ATT = 2, K_X1 = 3, K_FFN = 4, K_X2 = 5, K_COL = 6 };
constexpr size_t OFF_CNT = 4 * KB;
__host__ __device__ constexpr unsigned cnt_off(int kind, int c, int idx) { return (unsigned)(OFF_CNT + (size_t)((kind * 2 + c) * 32 + idx) * 128); }
constexpr size_t ZERO_BYTES = 64 * KB;
constexpr size_t ROW = 32 * KB;
constexpr size_t OFF_WQKV = 2 * MB;
constexpr size_t OFF_WO = OFF_WQKV + 12 * MB;
constexpr size_t OFF_WG = OFF_WO + 4 * MB;
constexpr size_t OFF_WU = OFF_WG + NFFN * 128 * KB;
constexpr size_t OFF_WD = OFF_WU + NFFN * 128 * KB;
constexpr size_t OFF_XA = 24 * MB;
constexpr size_t OFF_XF = OFF_XA + 4 * MB;
constexpr size_t OFF_KVM = OFF_XF + 4 * MB;
constexpr size_t OFF_QKVF = OFF_KVM + 16 * MB;
constexpr size_t OFF_PO = OFF_QKVF + 96 * MB;
constexpr size_t OFF_PF = OFF_PO + 32 * MB;
constexpr size_t OFF_P = OFF_PF + 28 * MB;
constexpr size_t CR_BLK = 16 * ROW, P_CHAIN = 16 * CR_BLK, M_ROW0 = 8 * ROW, M_SLOT = 2 * ROW;
constexpr size_t WS_BYTES = OFF_P + 2 * P_CHAIN + CR_BLK;
__device__ __forceinline__ unsigned p_region(const unsigned char* ws, int c) {
  return (unsigned)OFF_P + (unsigned)((0u - (unsigned)(uintptr_t)ws - (unsigned)OFF_P) & (unsigned)(CR_BLK - 1)) + (unsigned)c * (unsigned)P_CHAIN;
}
static_assert(OFF_CNT + 14 * 32 * 128 <= ZERO_BYTES, "flags");
static_assert(OFF_WD + NFFN * 128 * KB <= OFF_XA, "weights");
static_assert(WS_BYTES <= 256 * MB, "workspace");

constexpr unsigned kSpinCap = 1u << 17;
#define POLL 3
#define CAL_WAIT 0
#define CAL_N 2
#define LATE 2
#define CH_SLEEP 0
#define CR_SLEEP 56
constexpr int LDS_BYTES = 163840;

#define STAMP(s, i) do {} while (0)
#define STAMP0(s, i) do {} while (0)

#define TICKDECL do {} while (0)
#define TICKDUMP do {} while (0)
struct Params {
  const u16* x;
  const u16* mem0;
  const u16* wg;
  const u16* wu;
  const u16* wd;
  u16* out;
  unsigned char* ws;
};
struct PrepParams {
  const u16* x;
  const u16* wq;
  const u16* wk;
  const u16* wv;
  const u16* wo;
  unsigned char* ws;
};

__device__ __forceinline__ unsigned xcc_id() { return (unsigned)__builtin_amdgcn_s_getreg(20 | (3 << 11)) & 7u; }
__device__ __forceinline__ unsigned pack2(float a, float b) {
  f32x2 v = {a, b};
  bf16x2 r = __builtin_convertvector(v, bf16x2);
  return __builtin_bit_cast(unsigned, r);
}
__device__ __forceinline__ float bf16r(float a) { return __uint_as_float(pack2(a, 0.f) << 16); }
__device__ __forceinline__ float bflo(unsigned u) { return __uint_as_float(u << 16); }
__device__ __forceinline__ float bfhi(unsigned u) { return __uint_as_float(u & 0xffff0000u); }
#define MFMA "v_mfma_f32_16x16x32_bf16 "
template <bool FIRST> __device__ __forceinline__ void mfa(f32x4& c, v4u a, v4u b) {
  if (FIRST) asm(MFMA "%0, %1, %2, 0" : "=&v"(c) : "a"(a), "v"(b));
  else asm(MFMA "%0, %1, %2, %0" : "+v"(c) : "a"(a), "v"(b));
}
template <bool FIRST> __device__ __forceinline__ void mfv(f32x4& c, v4u a, v4u b) {
  if (FIRST) asm(MFMA "%0, %1, %2, 0" : "=&v"(c) : "v"(a), "v"(b));
  else asm(MFMA "%0, %1, %2, %0" : "+v"(c) : "v"(a), "v"(b));
}
#define MFA(first, c, a, b) do { if (first) mfa<true>(c, a, b); else mfa<false>(c, a, b); } while (0)
#define MFV(first, c, a, b) do { if (first) mfv<true>(c, a, b); else mfv<false>(c, a, b); } while (0)
__device__ __forceinline__ void mfma4a(f32x4& c0, f32x4& c1, f32x4& c2, f32x4& c3, v4u a0, v4u a1, v4u a2, v4u a3, v4u b) {
  asm("s_nop 1\n\t"
      "v_mfma_f32_16x16x32_bf16 %0, %4, %8, %0\n\t"
      "v_mfma_f32_16x16x32_bf16 %1, %5, %8, %1\n\t"
      "v_mfma_f32_16x16x32_bf16 %2, %6, %8, %2\n\t"
      "v_mfma_f32_16x16x32_bf16 %3, %7, %8, %3"
      : "+v"(c0), "+v"(c1), "+v"(c2), "+v"(c3)
      : "a"(a0), "a"(a1), "a"(a2), "a"(a3), "v"(b));
}
__device__ __forceinline__ void mfma6a(f32x4& c0, f32x4& c1, f32x4& c2, f32x4& c3, f32x4& c4, f32x4& c5,
                                       v4u a0, v4u a1, v4u a2, v4u a3, v4u b4, v4u b5, v4u b) {
  asm("s_nop 1\n\t"
      MFMA "%0, %6, %12, %0\n\t"
      MFMA "%1, %7, %12, %1\n\t"
      MFMA "%2, %8, %12, %2\n\t"
      MFMA "%3, %9, %12, %3\n\t"
      MFMA "%4, %12, %10, %4\n\t"
      MFMA "%5, %12, %11, %5"
      : "+v"(c0), "+v"(c1), "+v"(c2), "+v"(c3), "+v"(c4), "+v"(c5)
      : "a"(a0), "a"(a1), "a"(a2), "a"(a3), "v"(b4), "v"(b5), "v"(b));
}
__device__ __forceinline__ void mfma2v(f32x4& c0, f32x4& c1, v4u a, v4u b0, v4u b1) {
  asm("s_nop 1\n\t"
      MFMA "%0, %2, %3, %0\n\t"
      MFMA "%1, %2, %4, %1"
      : "+v"(c0), "+v"(c1)
      : "v"(a), "v"(b0), "v"(b1));
}
__device__ __forceinline__ void mfma4v(f32x4& c0, f32x4& c1, f32x4& c2, f32x4& c3, v4u a0, v4u a1, v4u a2, v4u a3, v4u b) {
  asm("s_nop 1\n\t"
      MFMA "%0, %4, %8, %0\n\t"
      MFMA "%1, %5, %8, %1\n\t"
      MFMA "%2, %6, %8, %2\n\t"
      MFMA "%3, %7, %8, %3"
      : "+v"(c0), "+v"(c1), "+v"(c2), "+v"(c3)
      : "v"(a0), "v"(a1), "v"(a2), "v"(a3), "v"(b));
}
__device__ __forceinline__ void mfma3v(f32x4& c0, f32x4& c1, f32x4& c2, v4u a0, v4u a1, v4u a2, v4u b) {
  asm("s_nop 1\n\t"
      MFMA "%0, %3, %6, %0\n\t"
      MFMA "%1, %4, %6, %1\n\t"
      MFMA "%2, %5, %6, %2"
      : "+v"(c0), "+v"(c1), "+v"(c2)
      : "v"(a0), "v"(a1), "v"(a2), "v"(b));
}
__device__ __forceinline__ void mfma_pv(f32x4& c0, f32x4& c1, v4u a0, v4u a1, v4u m, v4u p0, v4u pl, v4u ph) {
  asm("s_nop 1\n\t"
      MFMA "%0, %2, %5, 0\n\t"
      MFMA "%1, %3, %5, 0\n\t"
      MFMA "%0, %4, %6, %0\n\t"
      MFMA "%1, %4, %7, %1"
      : "=&v"(c0), "=&v"(c1)
      : "v"(a0), "v"(a1), "v"(m), "v"(p0), "v"(pl), "v"(ph));
}
__device__ __forceinline__ void mfma_k4(f32x4& c, v4u a0, v4u a1, v4u a2, v4u a3, v4u b0, v4u b1, v4u b2, v4u b3) {
  asm("s_nop 1\n\t"
      MFMA "%0, %1, %5, 0\n\t"
      MFMA "%0, %2, %6, %0\n\t"
      MFMA "%0, %3, %7, %0\n\t"
      MFMA "%0, %4, %8, %0"
      : "=&v"(c)
      : "v"(a0), "v"(a1), "v"(a2), "v"(a3), "v"(b0), "v"(b1), "v"(b2), "v"(b3));
}
__device__ __forceinline__ void mfma_k2(f32x4& c, v4u a, v4u b0, v4u b1) {
  asm("s_nop 1\n\t"
      MFMA "%0, %1, %2, 0\n\t"
      MFMA "%0, %1, %3, %0"
      : "=&v"(c)
      : "v"(a), "v"(b0), "v"(b1));
}
__device__ __forceinline__ void mfma_settle2(f32x4& c0, f32x4& c1) { asm("s_nop 7\n\ts_nop 1" : "+v"(c0), "+v"(c1)); }
__device__ __forceinline__ void mfma_settle3(f32x4& c0, f32x4& c1, f32x4& c2) { asm("s_nop 7\n\ts_nop 1" : "+v"(c0), "+v"(c1), "+v"(c2)); }
__device__ __forceinline__ void mfma_settle(f32x4& c0, f32x4& c1, f32x4& c2, f32x4& c3) {
  asm("s_nop 7\n\ts_nop 1" : "+v"(c0), "+v"(c1), "+v"(c2), "+v"(c3));
}
__device__ __forceinline__ v4u pack8(f32x4 a, f32x4 b) {
  v4u r;
  r.x = pack2(a.x, a.y); r.y = pack2(a.z, a.w); r.z = pack2(b.x, b.y); r.w = pack2(b.z, b.w);
  return r;
}
__device__ __forceinline__ float xmax(float x) {
  auto s = __builtin_amdgcn_permlane16_swap(__float_as_uint(x), __float_as_uint(x), false, false);
  x = fmaxf(__uint_as_float(s[0]), __uint_as_float(s[1]));
  auto t = __builtin_amdgcn_permlane32_swap(__float_as_uint(x), __float_as_uint(x), false, false);
  return fmaxf(__uint_as_float(t[0]), __uint_as_float(t[1]));
}
__device__ __forceinline__ float xsum(float x) {
  auto s = __builtin_amdgcn_permlane16_swap(__float_as_uint(x), __float_as_uint(x), false, false);
  x = __uint_as_float(s[0]) + __uint_as_float(s[1]);
  auto t = __builtin_amdgcn_permlane32_swap(__float_as_uint(x), __float_as_uint(x), false, false);
  return __uint_as_float(t[0]) + __uint_as_float(t[1]);
}
__device__ __forceinline__ unsigned ror8(unsigned x) {
  return (unsigned)__builtin_amdgcn_update_dpp(0, (int)x, 0x128, 0xf, 0xf, false);
}
__device__ __forceinline__ float ror8f(float x) { return __uint_as_float(ror8(__float_as_uint(x))); }
template <int CTRL> __device__ __forceinline__ float dppf(float x) {
  return __uint_as_float((unsigned)__builtin_amdgcn_update_dpp(0, (int)__float_as_uint(x), CTRL, 0xf, 0xf, false));
}
__device__ __forceinline__ float wave_sum(float x) {
  x += dppf<0x121>(x); x += dppf<0x122>(x); x += dppf<0x124>(x); x += dppf<0x128>(x);
  return xsum(x);
}
typedef __amdgpu_buffer_rsrc_t rsrc_t;
__device__ __forceinline__ v4u ld16(rsrc_t rs, unsigned off) { return __builtin_amdgcn_raw_buffer_load_b128(rs, (int)off, 0, 16); }
__device__ __forceinline__ v4u ld16s(rsrc_t rs, unsigned voff, unsigned soff) { return __builtin_amdgcn_raw_buffer_load_b128(rs, (int)voff, (int)soff, 16); }
__device__ __forceinline__ v2u ld8(rsrc_t rs, unsigned off) { return __builtin_amdgcn_raw_buffer_load_b64(rs, (int)off, 0, 16); }
__device__ __forceinline__ v2u ld8s(rsrc_t rs, unsigned voff, unsigned soff) { return __builtin_amdgcn_raw_buffer_load_b64(rs, (int)voff, (int)soff, 16); }
__device__ __forceinline__ unsigned ld4(rsrc_t rs, unsigned off) { return __builtin_amdgcn_raw_buffer_load_b32(rs, (int)off, 0, 16); }
__device__ __forceinline__ v4u ld16p(rsrc_t rs, unsigned off) { return __builtin_amdgcn_raw_buffer_load_b128(rs, (int)off, 0, 0); }
__device__ __forceinline__ void st16(rsrc_t rs, unsigned off, v4u v) { __builtin_amdgcn_raw_buffer_store_b128(v, rs, (int)off, 0, 16); }
__device__ __forceinline__ void st16s(rsrc_t rs, unsigned voff, unsigned soff, v4u v) { __builtin_amdgcn_raw_buffer_store_b128(v, rs, (int)voff, (int)soff, 16); }
__device__ __forceinline__ void st8(rsrc_t rs, unsigned off, v2u v) { __builtin_amdgcn_raw_buffer_store_b64(v, rs, (int)off, 0, 16); }
__device__ __forceinline__ void st4(rsrc_t rs, unsigned off, unsigned v) { __builtin_amdgcn_raw_buffer_store_b32(v, rs, (int)off, 0, 16); }

__device__ __forceinline__ bool spin_fail(unsigned& spins, rsrc_t rs) {
  ++spins;
  if (spins > kSpinCap) { st4(rs, (unsigned)OFF_ABORT, 1u); return true; }
  if ((spins & 127u) == 0u) return __builtin_amdgcn_readfirstlane(ld4(rs, (unsigned)OFF_ABORT)) != 0u;
  return false;
}
template <int SLEEP>
__device__ __forceinline__ void wait_cnt(rsrc_t rs, unsigned off, int n, int probe, unsigned want, int lane) {
  unsigned spins = 0;
  for (;;) {
    const unsigned v = ld4(rs, off + (unsigned)probe * 128u);
    if (__builtin_amdgcn_readfirstlane(v) >= want) break;
    if (spin_fail(spins, rs)) break;
    __builtin_amdgcn_s_sleep(SLEEP);
  }
  if (n > 1) {
    for (;;) {
      const unsigned v = ld4(rs, off + (unsigned)(lane < n ? lane : 0) * 128u);
      if (__all(v >= want)) break;
      if (spin_fail(spins, rs)) break;
      __builtin_amdgcn_s_sleep(2);
    }
  }
  __builtin_amdgcn_fence(__ATOMIC_ACQUIRE, "wavefront");
}

__device__ __forceinline__ unsigned wait_table(rsrc_t rs, int c, int lane) {
  unsigned spins = 0, v;
  for (;;) {
    v = ld4(rs, cnt_off(K_COL, c, 0) + (unsigned)(lane & 15) * 128u);
    if (__all(v != 0u)) break;
    if (spin_fail(spins, rs)) break;
    __builtin_amdgcn_s_sleep(8);
  }
  return v - 1u;
}
__device__ __forceinline__ unsigned mem_copy(unsigned tab, unsigned x) {
  const unsigned long long m = __ballot(((tab >> 3) & 7u) == x) & 0xffffull;
  const int own = m == 0ull ? 0 : __builtin_ctzll(m);
  return (unsigned)own * (unsigned)CR_BLK + (unsigned)M_ROW0 + ((unsigned)__builtin_amdgcn_readlane(tab, own) & 7u) * 4096u;
}

__device__ __forceinline__ void layernorm8(const float (&x)[8], float (&y)[8]) {
  float s = 0.f;
#pragma unroll
  for (int j = 0; j < 8; ++j) s += x[j];
  const float mean = wave_sum(s) * (1.0f / DM);
  float q = 0.f;
#pragma unroll
  for (int j = 0; j < 8; ++j) { const float d = x[j] - mean; q += d * d; }
  const float var = wave_sum(q) * (1.0f / DM);
  const float m = bf16r(mean);
  const float rden = 1.0f / bf16r(sqrtf(bf16r(bf16r(var) + 1e-5f)));
#pragma unroll
  for (int j = 0; j < 8; ++j) y[j] = bf16r(x[j] - m) * rden;
}

typedef short v4s __attribute__((ext_vector_type(4)));
__device__ __forceinline__ v2u tr8(const LAS unsigned char* a) {
  return __builtin_bit_cast(v2u, __builtin_amdgcn_ds_read_tr16_b64_v4i16((LAS v4s*)a));
}

constexpr int NJ_QKV = 3 * 16 * 16;
constexpr int NJ_WO = 32 * 4 * 2;
constexpr int NJ_LN = S_TOK / 4;
constexpr int NJ_INIT = 256;
constexpr int NJ_ALL = NJ_QKV + NJ_WO + NJ_LN + NJ_INIT;

__global__ void __launch_bounds__(256) k_prep(PrepParams p) {
  __shared__ __attribute__((aligned(16))) unsigned char tile[32 * 528];
  const int tid = threadIdx.x, lane = tid & 63, w = tid >> 6;
  int job = blockIdx.x;
  if (job < NJ_QKV + NJ_WO) {
    const bool pi = job >= NJ_QKV;
    const u16* src; int ld; size_t dst, tstride;
    if (!pi) {
      const int m = job >> 8, ks = (job >> 4) & 15, hp = job & 15;
      src = (m == 0 ? p.wq : (m == 1 ? p.wk : p.wv)) + (size_t)(32 * ks) * (NH * DH) + hp * 256; ld = NH * DH;
      dst = OFF_WQKV + ((size_t)(m * 32 + 2 * hp) * 8 * 16 + ks) * KB; tstride = 16 * KB;
    } else {
      job -= NJ_QKV;
      const int h = job >> 3, ks = (job >> 1) & 3, nb = job & 1;
      src = p.wo + (size_t)(h * DH + 32 * ks) * DM + nb * 256; ld = DM;
      dst = OFF_WO + ((size_t)(h * 32 + nb * 16) * 4 + ks) * KB; tstride = 4 * KB;
    }
    v4u v[4];
#pragma unroll
    for (int it = 0; it < 4; ++it) v[it] = *(const v4u*)(src + (size_t)(it * 8 + (tid >> 5)) * ld + (tid & 31) * 8);
#pragma unroll
    for (int it = 0; it < 4; ++it) *(v4u*)(tile + (it * 8 + (tid >> 5)) * 528 + (tid & 31) * 16) = v[it];
    __syncthreads();
    const int g = lane >> 4, q = (lane >> 2) & 3, pc = lane & 3;
    const LAS unsigned char* T = (const LAS unsigned char*)tile;
#pragma unroll
    for (int it = 0; it < 4; ++it) {
      const int t = w * 4 + it;
      const LAS unsigned char* a = pi ? T + (4 * g + q) * 528 + (t >> 1) * 64 + pc * 16 + (t & 1) * 8 : T + (8 * g + q) * 528 + t * 32 + pc * 8;
      const v2u lo = tr8(a), hi = tr8(a + (pi ? 16 : 4) * 528);
      *(v4u*)(p.ws + dst + (size_t)t * tstride + lane * 16) = v4u{lo.x, lo.y, hi.x, hi.y};
    }
    return;
  }
  job -= NJ_QKV + NJ_WO;
  if (job < NJ_LN) {
    const int row = job * 4 + w;
    const v4u v = *(const v4u*)(p.x + (size_t)row * DM + lane * 8);
    float x[8] = {bflo(v.x), bfhi(v.x), bflo(v.y), bfhi(v.y), bflo(v.z), bfhi(v.z), bflo(v.w), bfhi(v.w)}, y[8];
    layernorm8(x, y);
    v4u o; o.x = pack2(y[0], y[1]); o.y = pack2(y[2], y[3]); o.z = pack2(y[4], y[5]); o.w = pack2(y[6], y[7]);
    *(v4u*)(p.ws + OFF_XA + (size_t)row * 1024 + lane * 16) = o;
    return;
  }
  job -= NJ_LN;
  {
    const v4u z = {0u, 0u, 0u, 0u};
    for (size_t i = (size_t)job * 256 + tid; i < ZERO_BYTES / 16; i += (size_t)NJ_INIT * 256) *(v4u*)(p.ws + i * 16) = z;
    *(v4u*)(p.ws + p_region(p.ws, 0) + (size_t)(job >> 3) * CR_BLK + (job & 7) * 4096 + tid * 16) = z;
  }
}

__device__ __forceinline__ int hkey(int row) { return ((row >> 1) & 1) | ((row >> 2) & 2); }
__device__ __forceinline__ void half_issue(const u16* src, int ld, int t, LAS unsigned char* img, int w, int lane) {
  const int row = lane >> 3, pos = (lane >> 1) & 3;
#pragma unroll
  for (int i = 0; i < 16; ++i) {
    const int r0 = (4 * i + w) * 8 + row;
    const u16* q = src + (size_t)r0 * ld + (2 * (pos ^ hkey(r0)) + t) * 16 + (lane & 1) * 8;
    const unsigned dst = (unsigned)(uintptr_t)img + (unsigned)((4 * i + w) * 1024);
    unsigned keep;
    asm volatile("s_mov_b32 %0, m0\n\ts_mov_b32 m0, %2\n\ts_nop 0\n\tglobal_load_lds_dwordx4 %1, off\n\ts_mov_b32 m0, %0"
                 : "=&s"(keep) : "v"(q), "s"(dst) : "memory");
  }
  SB();
}
__device__ __forceinline__ v4u half_frag(const LAS unsigned char* img, int w, int ks, int lane) {
  const int row = 8 * (lane >> 4) + ((lane >> 2) & 3);
  const LAS unsigned char* a = img + (32 * ks + row) * 128 + ((w ^ hkey(row)) * 32) + (lane & 3) * 8;
  const v2u lo = tr8(a), hi = tr8(a + 512);
  return v4u{lo.x, lo.y, hi.x, hi.y};
}
#define RAWBAR() do { asm volatile("s_waitcnt lgkmcnt(0)" ::: "memory"); __builtin_amdgcn_s_barrier(); asm volatile("" ::: "memory"); } while (0)
template <int NH2, class SRC, class USE>
__device__ __forceinline__ void stream_halves(LAS unsigned char* buf, int ld, int w, int lane, SRC&& src, USE&& use) {
  half_issue(src(0), ld, 0, buf, w, lane);
  half_issue(src(0), ld, 1, buf + 65536, w, lane);
#pragma unroll
  for (int i = 0; i < NH2; ++i) {
    if (i + 1 < NH2) asm volatile("s_waitcnt vmcnt(16)" ::: "memory");
    else asm volatile("s_waitcnt vmcnt(0)" ::: "memory");
    RAWBAR();
    use(i, buf + (i & 1) * 65536);
    RAWBAR();
    if (i + 2 < NH2) half_issue(src((i + 2) >> 1), ld, i & 1, buf + (i & 1) * 65536, w, lane);
  }
}
constexpr int PSTG = 16 * 272;
template <class OUT>
__device__ __forceinline__ void wave_panel(const u16* src, int ld, LAS unsigned char* stg, int lane, OUT&& out) {
  const u16* q = src + (size_t)(lane >> 4) * ld + (lane & 15) * 8;
  const LAS unsigned char* a = stg + (4 * (lane >> 4) + ((lane >> 2) & 3)) * 272 + (lane & 3) * 16;
#pragma unroll
  for (int bt = 0; bt < 2; ++bt) {
    v4u v[16];
#pragma unroll
    for (int i = 0; i < 16; ++i) v[i] = *(const v4u*)(q + (size_t)(64 * bt + 4 * i) * ld);
    SB();
#pragma unroll
    for (int k2 = 0; k2 < 2; ++k2) {
      v2u lo[8];
#pragma unroll
      for (int hf = 0; hf < 2; ++hf) {
#pragma unroll
        for (int i = 0; i < 4; ++i) *(LAS v4u*)(stg + ((lane >> 4) + 4 * i) * 272 + (lane & 15) * 16) = v[8 * k2 + 4 * hf + i];
#pragma unroll
        for (int t = 0; t < 8; ++t) {
          const v2u x = tr8(a + (t >> 1) * 64 + (t & 1) * 8);
          if (hf == 0) lo[t] = x;
          else out(t, 2 * bt + k2, v4u{lo[t].x, lo[t].y, x.x, x.y});
        }
        LDSWAIT();
        SB();
      }
    }
  }
}

constexpr float kScale = 0.08838834764831845f;
constexpr float kNegBig = -3.0e38f;
constexpr float kLog2e = 1.4426950408889634f;

__device__ __forceinline__ void softmax_frags(f32x4 sx0, f32x4 sx1, f32x4 sm, unsigned ok0, unsigned ok1, bool okm, v4u& p0, v4u& p1) {
  unsigned u[6] = {pack2(sx0.x, sx0.y), pack2(sx0.z, sx0.w), pack2(sx1.x, sx1.y), pack2(sx1.z, sx1.w), pack2(sm.x, sm.y), pack2(sm.z, sm.w)};
  float a[12];
#pragma unroll
  for (int j = 0; j < 6; ++j) {
    u[j] = pack2(bflo(u[j]) * kScale, bfhi(u[j]) * kScale);
    a[2 * j] = bflo(u[j]); a[2 * j + 1] = bfhi(u[j]);
  }
#pragma unroll
  for (int r = 0; r < 4; ++r) {
    a[r] = ((ok0 >> r) & 1u) ? a[r] : kNegBig;
    a[4 + r] = ((ok1 >> r) & 1u) ? a[4 + r] : kNegBig;
    a[8 + r] = okm ? a[8 + r] : kNegBig;
  }
  float mx = fmaxf(fmaxf(fmaxf(a[0], a[1]), fmaxf(a[2], a[3])), fmaxf(fmaxf(a[4], a[5]), fmaxf(a[6], a[7])));
  mx = fmaxf(mx, fmaxf(fmaxf(a[8], a[9]), fmaxf(a[10], a[11])));
  mx = xmax(mx);
  const float nm = -mx * kLog2e;
#pragma unroll
  for (int j = 0; j < 12; ++j) a[j] = __builtin_amdgcn_exp2f(fmaf(a[j], kLog2e, nm));
  float sum = ((a[0] + a[1]) + (a[2] + a[3])) + ((a[4] + a[5]) + (a[6] + a[7])) + ((a[8] + a[9]) + (a[10] + a[11]));
  sum = xsum(sum);
  const float inv = __builtin_amdgcn_rcpf(sum);
  p0.x = pack2(a[0] * inv, a[1] * inv); p0.y = pack2(a[2] * inv, a[3] * inv);
  p0.z = pack2(a[4] * inv, a[5] * inv); p0.w = pack2(a[6] * inv, a[7] * inv);
  p1.x = pack2(a[8] * inv, a[9] * inv); p1.y = pack2(a[10] * inv, a[11] * inv);
  p1.z = 0u; p1.w = 0u;
}

__device__ __forceinline__ void rows_load(rsrc_t rs, unsigned goff, v4u (&v)[8], int w, int lane) {
#pragma unroll
  for (int i = 0; i < 8; ++i) v[i] = ld16s(rs, (unsigned)lane * 16u + (unsigned)(i & 3) * 1024u, goff + (unsigned)(8 * w + (i & 4)) * 1024u);
  SB();
}
constexpr int RSTR = 1040;
constexpr int IMG = 32 * RSTR;
__device__ __forceinline__ void rows_put(LAS unsigned char* L, const v4u (&v)[8], int w, int lane) {
#pragma unroll
  for (int i = 0; i < 8; ++i) *(LAS v4u*)(L + (8 * w + i) * RSTR + lane * 16) = v[i];
}
__device__ __forceinline__ v4u row_frag(const LAS unsigned char* L, int tt, int ks, int fb) {
  return *(const LAS v4u*)(L + fb + ks * 64 + tt * 16 * RSTR);
}
__device__ __forceinline__ void partial_put(LAS unsigned char* L, const f32x4 (&acc)[8][2], int tt, int w, int lane) {
  LAS unsigned char* B = L + (16 * tt + (lane & 15)) * RSTR + w * 256 + (lane >> 4) * 16;
#pragma unroll
  for (int m = 0; m < 4; ++m) *(LAS v4u*)(B + m * 64) = pack8(acc[2 * m][tt], acc[2 * m + 1][tt]);
}
__device__ __forceinline__ v4u partial_row(const LAS unsigned char* L, int tt, int k, int w, int lane) {
  return *(const LAS v4u*)(L + (16 * tt + 4 * w + k) * RSTR + lane * 16);
}
__device__ __forceinline__ void partial_row_store(rsrc_t rs, unsigned goff, v4u v, int tt, int k, int w, int lane) {
  st16s(rs, (unsigned)lane * 16u + (unsigned)k * 1024u, goff + (unsigned)(16 * tt + 4 * w) * 1024u, v);
}
__device__ __forceinline__ void partial_store(rsrc_t rs, unsigned goff, const LAS unsigned char* L, int tt, int w, int lane) {
  v4u v[4];
#pragma unroll
  for (int k = 0; k < 4; ++k) v[k] = partial_row(L, tt, k, w, lane);
#pragma unroll
  for (int k = 0; k < 4; ++k) partial_row_store(rs, goff, v[k], tt, k, w, lane);
  SB();
}

constexpr int CH_KV = 0, CH_MEM = 16384, CH_EX = CH_MEM + 8192, CH_WV = CH_EX + 6144, CH_FL = CH_WV + 131072 + 2032;
__device__ __forceinline__ void role_chain(const Params& p, rsrc_t rs, LAS unsigned char* L, int c, int h) {
  const int tid = threadIdx.x, lane = tid & 63, w = __builtin_amdgcn_readfirstlane(tid >> 6);
  const int g = lane >> 4, r = lane & 15, r8 = lane & 7;
  v4u wq[2][16], wk[2][16], wo[8][4];
#pragma unroll
  for (int t = 0; t < 2; ++t)
#pragma unroll
    for (int ks = 0; ks < 16; ++ks) {
      const unsigned o = (unsigned)(((h * 8 + 2 * w + t) * 16 + ((4 * w + ks) & 15)) * 1024 + lane * 16);
      wq[t][ks] = ld16p(rs, (unsigned)OFF_WQKV + o);
      wk[t][ks] = ld16p(rs, (unsigned)(OFF_WQKV + 4 * MB) + o);
    }
  for (int i = 0; i < 32; ++i) {
    const int t = i >> 4, j = i & 15;
    *(LAS v4u*)(L + CH_WV + ((2 * w + t) * 16 + j) * 1024 + lane * 16) =
        ld16p(rs, (unsigned)(OFF_WQKV + 8 * MB) + (unsigned)(((h * 8 + 2 * w + t) * 16 + ((4 * w + j) & 15)) * 1024 + lane * 16));
  }
#pragma unroll
  for (int dt = 0; dt < 8; ++dt)
#pragma unroll
    for (int ks = 0; ks < 4; ++ks) wo[dt][ks] = ld16p(rs, (unsigned)OFF_WO + (unsigned)(((h * 32 + 8 * w + dt) * 4 + ks) * 1024 + lane * 16));
  v4u mo[4];
#pragma unroll
  for (int j = 0; j < 4; ++j) {
    mo[j] = *(const v4u*)(p.mem0 + r8 * DM + (4 * w + j) * 32 + g * 8);
    if (r < 8) *(LAS v4u*)(L + CH_MEM + (4 * w + j) * 512 + g * 128 + r * 16) = mo[j];
  }
  const unsigned qkv_base = (unsigned)(OFF_QKVF + (size_t)c * NSEG * NH * 24 * KB) + (unsigned)h * 24u * 1024u + 8192u;
  const unsigned kvm_base = (unsigned)(OFF_KVM + (size_t)c * NSEG * NH * 4 * KB) + (unsigned)h * 4096u;
  {
    wait_cnt<8>(rs, cnt_off(K_QKV, c, h), 1, 0, 1u, lane);
    v4u v[4];
#pragma unroll
    for (int i = 0; i < 4; ++i) v[i] = ld16(rs, qkv_base + (unsigned)((4 * w + i) * 1024 + lane * 16));
    SB();
#pragma unroll
    for (int i = 0; i < 4; ++i) *(LAS v4u*)(L + CH_KV + (4 * w + i) * 1024 + lane * 16) = v[i];
  }
  unsigned p_off[2], m_base;
  {
    const unsigned tab = wait_table(rs, c, lane);
#pragma unroll
    for (int q = 0; q < 2; ++q) {
      const unsigned ca = (unsigned)__builtin_amdgcn_readlane(tab, 4 * w + 2 * q) & 7u, cb = (unsigned)__builtin_amdgcn_readlane(tab, 4 * w + 2 * q + 1) & 7u;
      p_off[q] = (unsigned)((4 * w + 2 * q + (r >> 3)) * CR_BLK + g * 2 * ROW + h * 128 + r8 * 16) + (r < 8 ? ca : cb) * 4096u;
    }
    m_base = p_region(p.ws, c) + mem_copy(tab, xcc_id()) + (unsigned)((w >> 1) * ROW + (w & 1) * 2048 + g * 128 + r8 * 16);
  }
  unsigned have_qkv = 1;
  TICKDECL;
  volatile LAS v4u* FL = (volatile LAS v4u*)(L + CH_FL);
  if (tid == 0) *FL = v4u{0u, 0u, 0u, 0u};
  __syncthreads();
  v4u wv0[2], wv1[2];
#pragma unroll
  for (int j = 0; j < 2; ++j) {
    wv0[j] = *(const LAS v4u*)(L + CH_WV + 2 * w * 16384 + lane * 16 + j * 1024);
    wv1[j] = *(const LAS v4u*)(L + CH_WV + 2 * w * 16384 + 16384 + lane * 16 + j * 1024);
  }
  for (int s = 0; s < NSEG; ++s) {
    STAMP0(s, 0);
    const LAS unsigned char* MF = L + CH_MEM + g * 128 + r8 * 16;
    const LAS unsigned char* WV = L + CH_WV + 2 * w * 16384 + lane * 16;
    f32x4 aq0, aq1, ak0, ak1, av0, av1;
    v4u kx[8];
    {
      v4u fl = *FL;
      v4u mf[4];
      SB();
#define QKV_STEP(j, m) do { \
        MFA((j) == 0, aq0, wq[0][j], m); MFA((j) == 0, aq1, wq[1][j], m); SB(); \
        if ((j) >= 4 && (j) + 3 < 16) mf[((j) + 3) & 3] = *(const LAS v4u*)(MF + ((w + (((j) + 3) >> 2)) & 3) * 2048 + (((j) + 3) & 3) * 512); \
        MFA((j) == 0, ak0, wk[0][j], m); MFA((j) == 0, ak1, wk[1][j], m); SB(); \
        if ((j) >= 12) kx[(((j) - 12) & 1) + 4 * (((j) - 12) >> 1)] = *(const LAS v4u*)(L + CH_KV + ((((j) - 12) & 1) + 4 * (((j) - 12) >> 1)) * 1024 + lane * 16); \
        MFV((j) == 0, av0, m, wv0[(j) & 1]); SB(); \
        if ((j) + 2 < 16) wv0[(j) & 1] = *(const LAS v4u*)(WV + ((j) + 2) * 1024); \
        MFV((j) == 0, av1, m, wv1[(j) & 1]); SB(); \
        if ((j) + 2 < 16) wv1[(j) & 1] = *(const LAS v4u*)(WV + 16384 + ((j) + 2) * 1024); \
      } while (0)
      QKV_STEP(0, mo[0]);
      QKV_STEP(1, mo[1]);
      for (unsigned spins = 0; __builtin_amdgcn_readfirstlane(min(min(fl.x, fl.y), min(fl.z, fl.w))) < (unsigned)s && spins < (1u << 22); ++spins) fl = *FL;
#pragma unroll
      for (int i = 0; i < 3; ++i) mf[i] = *(const volatile LAS v4u*)(MF + ((w + 1) & 3) * 2048 + i * 512);
      SB();
      QKV_STEP(2, mo[2]);
      QKV_STEP(3, mo[3]);
#pragma unroll
      for (int j = 4; j < 16; ++j) { const v4u m = mf[j & 3]; QKV_STEP(j, m); }
#undef QKV_STEP
    }
    asm("s_nop 7\n\ts_nop 1" : "+v"(aq0), "+v"(aq1), "+v"(ak0), "+v"(ak1), "+v"(av0), "+v"(av1));
    {
      const v4u kq = r < 8 ? pack8(ak0, ak1) : pack8(aq0, aq1);
      *(LAS v4u*)(L + CH_EX + (r >> 3) * 4096 + w * 512 + g * 128 + r8 * 16) = kq;
      *(LAS v4u*)(L + CH_EX + 2048 + w * 512 + (g & 1) * 256 + r * 16) = pack8(av0, av1);
    }
    __syncthreads();
    STAMP0(s, 1);
    const LAS unsigned char* EXL = L + CH_EX + g * 128 + r8 * 16;
    f32x4 sx0, sx1, sm;
    {
      v4u qf[4], km[2];
#pragma unroll
      for (int i = 0; i < 4; ++i) qf[i] = *(const LAS v4u*)(EXL + 4096 + i * 512);
#pragma unroll
      for (int i = 0; i < 2; ++i) km[i] = *(const LAS v4u*)(EXL + i * 512);
#pragma unroll
      for (int i = 2; i < 4; ++i) kx[i] = *(const LAS v4u*)(L + CH_KV + i * 1024 + lane * 16);
      SB();
#pragma unroll
      for (int i = 0; i < 2; ++i) {
        MFV(i == 0, sx0, kx[i], qf[i]); MFV(i == 0, sx1, kx[4 + i], qf[i]);
        SB();
        kx[6 + i] = *(const LAS v4u*)(L + CH_KV + (6 + i) * 1024 + lane * 16);
        MFV(i == 0, sm, km[i], qf[i]);
        SB();
        km[i] = *(const LAS v4u*)(EXL + (2 + i) * 512);
      }
#pragma unroll
      for (int i = 2; i < 4; ++i) MFV(false, sx0, kx[i], qf[i]);
#pragma unroll
      for (int i = 2; i < 4; ++i) { MFV(false, sx1, kx[4 + i], qf[i]); MFV(false, sm, km[i - 2], qf[i]); }
      SB();
    }
    v4u vx[4], vma[4];
#pragma unroll
    for (int et = 0; et < 4; ++et) vx[et] = *(const LAS v4u*)(L + CH_KV + 8192 + et * 1024 + lane * 16);
#pragma unroll
    for (int m = 0; m < 4; ++m) vma[m] = *(const LAS v4u*)(L + CH_EX + 2048 + m * 512 + (g & 1) * 256 + r * 16);
    SB();
    mfma_settle3(sx0, sx1, sm);
    v4u p0, p1;
    {
      const bool lo = r < 8;
      const f32x4 sa = lo ? sx0 : sx1;
      unsigned u0 = pack2(sa.x, sa.y), u1 = pack2(sa.z, sa.w), u2 = pack2(lo ? sm.x : sm.z, lo ? sm.y : sm.w);
      u0 = pack2(bflo(u0) * kScale, bfhi(u0) * kScale);
      u1 = pack2(bflo(u1) * kScale, bfhi(u1) * kScale);
      u2 = pack2(bflo(u2) * kScale, bfhi(u2) * kScale);
      float a0 = bflo(u0), a1 = bfhi(u0), a2 = bflo(u1), a3 = bfhi(u1);
      float a4 = g < 2 ? bflo(u2) : kNegBig, a5 = g < 2 ? bfhi(u2) : kNegBig;
      float mx = fmaxf(fmaxf(fmaxf(a0, a1), fmaxf(a2, a3)), fmaxf(a4, a5));
      mx = fmaxf(mx, ror8f(mx));
      mx = xmax(mx);
      const float nm = -mx * kLog2e;
      a0 = __builtin_amdgcn_exp2f(fmaf(a0, kLog2e, nm)); a1 = __builtin_amdgcn_exp2f(fmaf(a1, kLog2e, nm));
      a2 = __builtin_amdgcn_exp2f(fmaf(a2, kLog2e, nm)); a3 = __builtin_amdgcn_exp2f(fmaf(a3, kLog2e, nm));
      a4 = __builtin_amdgcn_exp2f(fmaf(a4, kLog2e, nm)); a5 = __builtin_amdgcn_exp2f(fmaf(a5, kLog2e, nm));
      float sum = ((a0 + a1) + (a2 + a3)) + (a4 + a5);
      sum += ror8f(sum);
      sum = xsum(sum);
      const float inv = __builtin_amdgcn_rcpf(sum);
      u0 = pack2(a0 * inv, a1 * inv); u1 = pack2(a2 * inv, a3 * inv); u2 = pack2(a4 * inv, a5 * inv);
      const unsigned o0 = ror8(u0), o1 = ror8(u1), o2 = ror8(u2);
      p0.x = lo ? u0 : o0; p0.y = lo ? u1 : o1; p0.z = lo ? o0 : u0; p0.w = lo ? o1 : u1;
      p1.x = lo ? u2 : o2; p1.y = lo ? o2 : u2; p1.z = 0u; p1.w = 0u;
    }
    const v4u p1h = {0u, 0u, p1.x, p1.y};
    STAMP0(s, 6);
    if (s + 1 < NSEG) {
      v4u of[4];
      f32x4 o2[8], pa[8];
      unsigned pk[8][2];
      const unsigned soff = p_region(p.ws, c) + (unsigned)((s & 1) * ROW);
      mfma_pv(o2[0], o2[1], vx[0], vx[1], vma[0], p0, p1, p1h);
      SB();
      vx[0] = *(const LAS v4u*)(L + CH_KV + 8192 + 4 * 1024 + lane * 16);
      vx[1] = *(const LAS v4u*)(L + CH_KV + 8192 + 5 * 1024 + lane * 16);
      mfma_pv(o2[2], o2[3], vx[2], vx[3], vma[1], p0, p1, p1h);
      SB();
      vx[2] = *(const LAS v4u*)(L + CH_KV + 8192 + 6 * 1024 + lane * 16);
      vx[3] = *(const LAS v4u*)(L + CH_KV + 8192 + 7 * 1024 + lane * 16);
      mfma_pv(o2[4], o2[5], vx[0], vx[1], vma[2], p0, p1, p1h);
      SB();
      of[0] = pack8(o2[0], o2[1]);
      SB();
      mfma_pv(o2[6], o2[7], vx[2], vx[3], vma[3], p0, p1, p1h);
      SB();
      of[1] = pack8(o2[2], o2[3]);
      SB();
#pragma unroll
      for (int t = 0; t < 8; ++t) {
        MFV(true, pa[t], wo[t][0], of[0]);
        SB();
        if (t == 4) { of[2] = pack8(o2[4], o2[5]); SB(); }
      }
#pragma unroll
      for (int t = 0; t < 8; ++t) {
        MFV(false, pa[t], wo[t][1], of[1]);
        SB();
        if (t == 2) { of[3] = pack8(o2[6], o2[7]); SB(); }
      }
#define PACK_TILE(t) do { pk[t][0] = min(pack2(pa[t].x, pa[t].y), 0xfffffffeu); pk[t][1] = min(pack2(pa[t].z, pa[t].w), 0xfffffffeu); \
                          asm("" : "+v"(pk[t][0]), "+v"(pk[t][1])); SB(); } while (0)
#pragma unroll
      for (int t = 0; t < 8; ++t) {
        MFV(false, pa[t], wo[t][2], of[2]);
        MFV(false, pa[t], wo[t][3], of[3]);
        SB();
        if (t >= 4) PACK_TILE(t - 4);
      }
      {
        const v4u o = {r < 8 ? pk[0][0] : pk[2][0], r < 8 ? pk[0][1] : pk[2][1], r < 8 ? pk[1][0] : pk[3][0], r < 8 ? pk[1][1] : pk[3][1]};
        st16s(rs, p_off[0], soff, o);
        SB();
      }
      PACK_TILE(4);
      PACK_TILE(5);
      asm("s_nop 1" : "+v"(pa[6]), "+v"(pa[7]));
      PACK_TILE(6);
      PACK_TILE(7);
#undef PACK_TILE
      const v4u o = {r < 8 ? pk[4][0] : pk[6][0], r < 8 ? pk[4][1] : pk[6][1], r < 8 ? pk[5][0] : pk[7][0], r < 8 ? pk[5][1] : pk[7][1]};
      st16s(rs, p_off[1], soff, o);
      SB();
    }
    STAMP0(s, 2);
    st16(rs, kvm_base + (unsigned)(s * NH * 4096 + w * 1024 + lane * 16), *(const LAS v4u*)(L + CH_EX + w * 1024 + lane * 16));
    __syncthreads();
    const bool more = s + 1 < NSEG;
    const bool kv_known = more && have_qkv >= (unsigned)(s + 2);
    if (kv_known) {
      v4u v[4];
#pragma unroll
      for (int i = 0; i < 4; ++i) v[i] = ld16s(rs, (unsigned)(i * 1024 + lane * 16), qkv_base + (unsigned)((s + 1) * NH * 24 * 1024 + 4 * w * 1024));
      SB();
#pragma unroll
      for (int i = 0; i < 4; ++i) *(LAS v4u*)(L + CH_KV + (4 * w + i) * 1024 + lane * 16) = v[i];
    }
    DRAIN();
    __syncthreads();
    if (tid == 0) st4(rs, cnt_off(K_KM, c, h), (unsigned)(s + 1));
    STAMP0(s, 3);
    if (!more) { TICKDUMP; break; }
    {
      if (!kv_known) {
        unsigned spins = 0;
        for (;;) {
          have_qkv = __builtin_amdgcn_readfirstlane(ld4(rs, cnt_off(K_QKV, c, h)));
          if (have_qkv >= (unsigned)(s + 2)) break;
          if (spin_fail(spins, rs)) break;
          __builtin_amdgcn_s_sleep(4);
        }
        __builtin_amdgcn_fence(__ATOMIC_ACQUIRE, "wavefront");
        v4u v[4];
#pragma unroll
        for (int i = 0; i < 4; ++i) v[i] = ld16s(rs, (unsigned)(i * 1024 + lane * 16), qkv_base + (unsigned)((s + 1) * NH * 24 * 1024 + 4 * w * 1024));
        SB();
#pragma unroll
        for (int i = 0; i < 4; ++i) *(LAS v4u*)(L + CH_KV + (4 * w + i) * 1024 + lane * 16) = v[i];
      } else if (have_qkv < (unsigned)(s + 3)) {
        have_qkv = __builtin_amdgcn_readfirstlane(ld4(rs, cnt_off(K_QKV, c, h)));
        __builtin_amdgcn_fence(__ATOMIC_ACQUIRE, "wavefront");
      }
      STAMP0(s, 4);
#pragma unroll
      for (int j = 0; j < 2; ++j) {
        wv0[j] = *(const LAS v4u*)(L + CH_WV + 2 * w * 16384 + lane * 16 + j * 1024);
        wv1[j] = *(const LAS v4u*)(L + CH_WV + 2 * w * 16384 + 16384 + lane * 16 + j * 1024);
      }
      if (kv_known) __builtin_amdgcn_s_sleep(CH_SLEEP);
      const unsigned slot = (unsigned)(((s + 1) % 3) * M_SLOT);
      unsigned spins = 0;
      for (;;) {
#pragma unroll
        for (int j = 0; j < 4; ++j) mo[j] = ld16s(rs, m_base + (unsigned)(j * 512), slot);
        SB();
        unsigned mx = 0;
#pragma unroll
        for (int j = 0; j < 4; ++j) mx = max(mx, max(max(mo[j].x, mo[j].y), max(mo[j].z, mo[j].w)));
        if (__all(mx != 0xffffffffu)) break;
        if (spin_fail(spins, rs)) break;
      }
      STAMP0(s, 5);
      if (r < 8) {
#pragma unroll
        for (int j = 0; j < 4; ++j) *(LAS v4u*)(L + CH_MEM + (4 * w + j) * 512 + g * 128 + r * 16) = mo[j];
      }
      LDSWAIT();
      if (lane == 0) ((volatile LAS unsigned*)FL)[w] = (unsigned)(s + 1);
      asm volatile("" ::: "memory");
    }
  }
}

__device__ __forceinline__ void role_creduce(const Params& p, rsrc_t rs, LAS unsigned char* L, int c, int idx) {
  const int tid = threadIdx.x, lane = tid & 63, w = __builtin_amdgcn_readfirstlane(tid >> 6);
  const unsigned l16 = (unsigned)lane * 16u;
  const unsigned xcc = xcc_id();
  const unsigned blk = p_region(p.ws, c) + (unsigned)(idx * CR_BLK);
  unsigned col;
  {
    volatile LAS unsigned* T = (volatile LAS unsigned*)L;
    if (w == 0) {
      for (int i = 0; i < (c == 0 ? CAL_WAIT : 4); ++i) __builtin_amdgcn_s_sleep(127);
      unsigned best = 0xffffffffu, arg = 0;
#pragma unroll 1
      for (int q = 0; q < 8 * (c == 0 ? CAL_N : 3); ++q) {
        const unsigned t0 = (unsigned)__builtin_amdgcn_s_memtime();
        unsigned y = ld4(rs, blk + (unsigned)((q & 7) * 4096 + (q >> 3) * 1024));
        asm volatile("s_waitcnt vmcnt(0)" : "+v"(y) :: "memory");
        const unsigned t1 = (unsigned)__builtin_amdgcn_s_memtime();
        if (t1 - t0 < best) { best = t1 - t0; arg = (unsigned)q & 7u; }
      }
      if (lane == 0) T[0] = arg;
    }
    __syncthreads();
    col = __builtin_amdgcn_readfirstlane(T[0]);
    const v4u o = {~0u, ~0u, ~0u, ~0u};
#pragma unroll
    for (int k = 0; k < 8; ++k) st16s(rs, l16 + (unsigned)(k & 3) * 1024u, blk + (unsigned)((2 * w + (k >> 2)) * ROW) + col * 4096u, o);
#pragma unroll
    for (int k = 0; k < 6; ++k) st16s(rs, l16 + (unsigned)w * 1024u, blk + (unsigned)(M_ROW0 + k * ROW) + col * 4096u, o);
    DRAIN();
    __syncthreads();
    if (tid == 0) st4(rs, cnt_off(K_COL, c, idx), 1u + col + 8u * xcc);
  }
  const unsigned p_base = blk + (unsigned)(2 * w * ROW) + col * 4096u;
  v4u sel;
  {
    const unsigned one = (lane & 1) ? 0x3f800000u : 0x00003f80u, q = ((unsigned)lane & 7u) >> 1;
    sel.x = q == 0u ? one : 0u; sel.y = q == 1u ? one : 0u; sel.z = q == 2u ? one : 0u; sel.w = q == 3u ? one : 0u;
  }
  unsigned m_off0, m_off1;
  {
    const unsigned tab = wait_table(rs, c, lane);
    const unsigned x0 = 2u * ((unsigned)lane >> 5) + (((unsigned)lane >> 3) & 1u);
    unsigned c0 = 0, c1 = 0;
#pragma unroll
    for (unsigned x = 0; x < 4; ++x) {
      const unsigned a = mem_copy(tab, x), b = mem_copy(tab, x + 4u);
      c0 = x0 == x ? a : c0; c1 = x0 == x ? b : c1;
    }
    const unsigned m_in = p_region(p.ws, c) + (unsigned)((idx >> 3) * ROW + (idx & 7) * 512 + w * 128 + (lane & 7) * 16 + ((lane >> 4) & 1) * 8);
    m_off0 = m_in + c0;
    m_off1 = m_in + c1;
  }
  for (int s = 0; s + 1 < NSEG; ++s) {
    const unsigned pb = p_base + (unsigned)((s & 1) * ROW);
    v4u x[4]; unsigned spins = 0;
    if (s > 0) __builtin_amdgcn_s_sleep(CR_SLEEP);
    for (;;) {
#pragma unroll
      for (int k = 0; k < 4; ++k) x[k] = ld16s(rs, l16 + (unsigned)k * 1024u, pb);
      SB();
      unsigned mx = 0;
#pragma unroll
      for (int k = 0; k < 4; ++k) mx = max(mx, max(max(x[k].x, x[k].y), max(x[k].z, x[k].w)));
      if (__all(mx != 0xffffffffu)) break;
      if (spin_fail(spins, rs)) break;
    }
    STAMP(s, w);
    f32x4 a, a2;
    mfma_k2(a, sel, x[0], x[1]);
    mfma_k2(a2, sel, x[2], x[3]);
    mfma_settle2(a, a2);
    a += a2;
    a.x += ror8f(a.x); a.y += ror8f(a.y); a.z += ror8f(a.z); a.w += ror8f(a.w);
    v2u o; o.x = min(pack2(a.x, a.y), 0xfffffffeu); o.y = min(pack2(a.z, a.w), 0xfffffffeu);
    const unsigned slot = (unsigned)(((s + 1) % 3) * M_SLOT);
    st8(rs, m_off0 + slot, o);
    st8(rs, m_off1 + slot, o);
    __builtin_amdgcn_s_sleep(16);
    const v4u ones4 = {~0u, ~0u, ~0u, ~0u};
#pragma unroll
    for (int k = 0; k < 4; ++k) st16s(rs, l16 + (unsigned)k * 1024u, pb, ones4);
    const unsigned old = (unsigned)((s % 3) * M_SLOT);
    const v2u ones = {~0u, ~0u};
    st8(rs, m_off0 + old, ones);
    st8(rs, m_off1 + old, ones);
  }
}

__device__ __forceinline__ void role_qkv(const Params& p, rsrc_t rs, LAS unsigned char* L, int c, int h) {
  const int tid = threadIdx.x, lane = tid & 63, w = __builtin_amdgcn_readfirstlane(tid >> 6);
  const int g = lane >> 4, r = lane & 15;
  v4u wq[2][16], wk[2][16], wv[2][16];
#pragma unroll
  for (int t = 0; t < 2; ++t)
#pragma unroll
    for (int ks = 0; ks < 16; ++ks) {
      const unsigned o = (unsigned)(((h * 8 + 2 * w + t) * 16 + ks) * 1024 + lane * 16);
      wq[t][ks] = ld16p(rs, (unsigned)OFF_WQKV + o);
      wk[t][ks] = ld16p(rs, (unsigned)(OFF_WQKV + 4 * MB) + o);
      wv[t][ks] = ld16p(rs, (unsigned)(OFF_WQKV + 8 * MB) + o);
    }
  const int fb = r * RSTR + g * 16;
  volatile LAS unsigned* RDY = (volatile LAS unsigned*)(L + 2 * IMG);
  const unsigned xbase = (unsigned)(OFF_XA + (size_t)c * 2 * MB);
  bool staged = false;
  TICKDECL;
  for (int s = 0; s < NSEG; ++s) {
    LAS unsigned char* X = L + (s & 1) * IMG;
    if (!staged) {
      if (c == 1) wait_cnt<POLL>(rs, cnt_off(K_X2, 0, 8 * w), 8, h & 7, (unsigned)(s + 1), lane);
      STAMP0(s, 0);
      v4u v[8];
      rows_load(rs, xbase + (unsigned)s * 32768u, v, w, lane);
      rows_put(X, v, w, lane);
      __syncthreads();
    }
    STAMP0(s, 1);
    unsigned nx = 0xffffffffu;
    if (c == 1 && w == 0) nx = ld4(rs, cnt_off(K_X2, 0, 0) + (unsigned)(lane & 31) * 128u);
    v4u nv[8];
    if (c == 0 && s + 1 < NSEG) rows_load(rs, xbase + (unsigned)(s + 1) * 32768u, nv, w, lane);
    f32x4 aq[2][2], ak[2][2], av[2][2];
    {
      v4u xf[4];
#pragma unroll
      for (int i = 0; i < 4; ++i) xf[i] = row_frag(X, i & 1, i >> 1, fb);
      SB();
#pragma unroll
      for (int i = 0; i < 32; ++i) {
        const int ks = i >> 1, tt = i & 1;
        const v4u x = xf[i & 3];
        MFA(ks == 0, aq[0][tt], wq[0][ks], x);
        MFA(ks == 0, aq[1][tt], wq[1][ks], x);
        MFA(ks == 0, ak[0][tt], wk[0][ks], x);
        SB();
        if (i >= 1 && i + 3 < 32) xf[(i + 3) & 3] = row_frag(X, (i + 3) & 1, (i + 3) >> 1, fb);
        MFA(ks == 0, ak[1][tt], wk[1][ks], x);
        MFV(ks == 0, av[0][tt], x, wv[0][ks]);
        MFV(ks == 0, av[1][tt], x, wv[1][ks]);
        SB();
      }
    }
    mfma_settle(av[0][0], av[1][0], av[0][1], av[1][1]);
    mfma_settle(aq[0][0], aq[1][0], ak[0][0], ak[1][0]);
    mfma_settle(aq[0][1], aq[1][1], ak[0][1], ak[1][1]);
    STAMP0(s, 2);
    const unsigned ob = (unsigned)(OFF_QKVF + (size_t)((c * NSEG + s) * NH + h) * 24 * KB) + (unsigned)lane * 16u;
#pragma unroll
    for (int tt = 0; tt < 2; ++tt) {
      st16(rs, ob + (unsigned)(8192 + (tt * 4 + w) * 1024), pack8(ak[0][tt], ak[1][tt]));
      st16(rs, ob + (unsigned)((tt * 4 + w) * 1024), pack8(aq[0][tt], aq[1][tt]));
    }
#pragma unroll
    for (int t = 0; t < 2; ++t) st16(rs, ob + (unsigned)(16384 + (2 * w + t) * 1024), pack8(av[t][0], av[t][1]));
    if (c == 0) {
      if (s + 1 < NSEG) rows_put(L + ((s + 1) & 1) * IMG, nv, w, lane);
      staged = true;
    } else if (w == 0) {
      const bool ok = __all(nx >= (unsigned)(s + 2)) && s + 1 < NSEG;
      if (lane == 0) RDY[0] = ok ? 1u : 0u;
    }
    DRAIN();
    __syncthreads();
    if (tid == 0) st4(rs, cnt_off(K_QKV, c, h), (unsigned)(s + 1));
    STAMP0(s, 3);
    if (c == 1) {
      staged = RDY[0] != 0u;
      if (staged) {
        v4u v[8];
        rows_load(rs, xbase + (unsigned)(s + 1) * 32768u, v, w, lane);
        rows_put(L + ((s + 1) & 1) * IMG, v, w, lane);
        __syncthreads();
      }
    }
  }
}

__device__ __forceinline__ void role_att(const Params& p, rsrc_t rs, LAS unsigned char* L, int c, int pp) {
  const int tid = threadIdx.x, lane = tid & 63, w = __builtin_amdgcn_readfirstlane(tid >> 6);
  const int g = lane >> 4, r = lane & 15, r8 = lane & 7;
  v4u wo[2][8][4];
#pragma unroll
  for (int hh = 0; hh < 2; ++hh)
#pragma unroll
    for (int dt = 0; dt < 8; ++dt)
#pragma unroll
      for (int ks = 0; ks < 4; ++ks)
        wo[hh][dt][ks] = ld16p(rs, (unsigned)OFF_WO + (unsigned)((((2 * pp + hh) * 32 + 8 * w + dt) * 4 + ks) * 1024 + lane * 16));
  const int hh = w >> 1, tt = w & 1, h = 2 * pp + hh;
  const int q = 16 * tt + r;
  unsigned ok0 = 0, ok1 = 0;
#pragma unroll
  for (int j = 0; j < 4; ++j) { ok0 |= (4 * g + j <= q ? 1u : 0u) << j; ok1 |= (16 + 4 * g + j <= q ? 1u : 0u) << j; }
  LAS unsigned char* EX = L;
  LAS unsigned char* TR = L + 16384;
  const unsigned qbase = (unsigned)(OFF_QKVF + (size_t)(c * NSEG * NH + h) * 24 * KB);
  const unsigned mbase = (unsigned)(OFF_KVM + (size_t)(c * NSEG * NH + h) * 4 * KB);
  const unsigned l16 = (unsigned)lane * 16u;
  unsigned have_qkv = 0, have_km = 0;
  v4u qf[4], kf[8], vf[8];
  bool loaded = false;
  TICKDECL;
  for (int s = 0; s < NSEG; ++s) {
    if (!loaded) {
      if (have_qkv < (unsigned)(s + 1)) wait_cnt<8>(rs, cnt_off(K_QKV, c, h), 1, 0, (unsigned)(s + 1), lane);
      const unsigned ib = qbase + (unsigned)(s * NH * 24 * 1024);
#pragma unroll
      for (int ks = 0; ks < 4; ++ks) qf[ks] = ld16s(rs, l16 + (unsigned)(ks * 1024), ib + (unsigned)(tt * 4096));
#pragma unroll
      for (int i = 0; i < 8; ++i) kf[i] = ld16s(rs, l16 + (unsigned)((i & 3) * 1024), ib + (unsigned)(8192 + (i & 4) * 1024));
#pragma unroll
      for (int i = 0; i < 8; ++i) vf[i] = ld16s(rs, l16 + (unsigned)((i & 3) * 1024), ib + (unsigned)(16384 + (i & 4) * 1024));
      SB();
    }
    f32x4 sx0, sx1, sm;
#pragma unroll
    for (int ks = 0; ks < 4; ++ks) { MFV(ks == 0, sx0, kf[ks], qf[ks]); MFV(ks == 0, sx1, kf[4 + ks], qf[ks]); }
    SB();
    if (have_km < (unsigned)(s + 1)) wait_cnt<POLL>(rs, cnt_off(K_KM, c, h), 1, 0, (unsigned)(s + 1), lane);
    STAMP0(s, 0);
    const unsigned mb = mbase + (unsigned)(s * NH * 4096);
    v4u km[4], vma[4];
#pragma unroll
    for (int ks = 0; ks < 4; ++ks) km[ks] = ld16s(rs, (unsigned)(ks * 512 + g * 128 + r8 * 16), mb);
#pragma unroll
    for (int m = 0; m < 4; ++m) vma[m] = ld16s(rs, (unsigned)(2048 + m * 512 + (g & 1) * 256 + r * 16), mb);
    const unsigned nq = ld4(rs, cnt_off(K_QKV, c, h));
    SB();
#pragma unroll
    for (int ks = 0; ks < 4; ++ks) MFV(ks == 0, sm, km[ks], qf[ks]);
    mfma_settle3(sx0, sx1, sm);
    STAMP0(s, 4);
    v4u p0, p1;
    softmax_frags(sx0, sx1, sm, ok0, ok1, g < 2, p0, p1);
    const v4u p1h = {0u, 0u, p1.x, p1.y};
    STAMP0(s, 5);
    {
      f32x4 o2[8];
#pragma unroll
      for (int m = 0; m < 4; ++m)
        mfma_pv(o2[2 * m], o2[2 * m + 1], vf[2 * m], vf[2 * m + 1], vma[m], p0, p1, p1h);
      mfma_settle(o2[0], o2[1], o2[2], o2[3]);
      mfma_settle(o2[4], o2[5], o2[6], o2[7]);
#pragma unroll
      for (int m = 0; m < 4; ++m) *(LAS v4u*)(EX + (w * 4 + m) * 1024 + lane * 16) = pack8(o2[2 * m], o2[2 * m + 1]);
    }
    have_qkv = __builtin_amdgcn_readfirstlane(nq);
    loaded = s + 1 < NSEG && have_qkv >= (unsigned)(s + 2);
    STAMP0(s, 6);
    __syncthreads();
    STAMP0(s, 7);
    f32x4 acc[8][2];
    const unsigned pout = (unsigned)(OFF_PO + (size_t)(s * NATT + pp) * 32 * KB);
    {
      const unsigned ib = qbase + (unsigned)((loaded ? s + 1 : s) * NH * 24 * 1024);
      v4u of[4], rw;
#define OFRAG(i) ((((i) >> 2) & 1) * 8 + ((i) >> 3) * 4 + ((i) & 3))
#pragma unroll
      for (int i = 0; i < 4; ++i) of[i] = *(const LAS v4u*)(EX + OFRAG(i) * 1024 + lane * 16);
      SB();
#pragma unroll
      for (int i = 0; i < 16; ++i) {
        const int t2 = i >> 3, h2 = (i >> 2) & 1, ks = i & 3;
        const bool first = h2 == 0 && ks == 0;
        const v4u x = of[i & 3];
#pragma unroll
        for (int dt = 0; dt < 4; ++dt) MFA(first, acc[dt][t2], wo[h2][dt][ks], x);
        SB();
        if (i >= 1 && i + 3 < 16) of[(i + 3) & 3] = *(const LAS v4u*)(EX + OFRAG(i + 3) * 1024 + lane * 16);
        if (i == 9) partial_put(TR, acc, 0, w, lane);
        if (i >= 11 && i < 15) rw = partial_row(TR, 0, i - 11, w, lane);
        MFA(first, acc[4][t2], wo[h2][4][ks], x);
        MFA(first, acc[5][t2], wo[h2][5][ks], x);
        SB();
        if (i < 4) qf[i] = ld16s(rs, l16 + (unsigned)(i * 1024), ib + (unsigned)(tt * 4096));
        else if (i < 12) kf[i - 4] = ld16s(rs, l16 + (unsigned)(((i - 4) & 3) * 1024), ib + (unsigned)(8192 + ((i - 4) & 4) * 1024));
        else vf[i - 12] = ld16s(rs, l16 + (unsigned)((i - 12) * 1024), ib + 16384u);
        MFA(first, acc[6][t2], wo[h2][6][ks], x);
        SB();
        if (i >= 12) vf[i - 8] = ld16s(rs, l16 + (unsigned)((i - 12) * 1024), ib + 20480u);
        if (i >= 11 && i < 15) partial_row_store(rs, pout, rw, 0, i - 11, w, lane);
        MFA(first, acc[7][t2], wo[h2][7][ks], x);
        SB();
        if (i == 10) __syncthreads();
      }
#undef OFRAG
    }
    mfma_settle(acc[0][1], acc[1][1], acc[2][1], acc[3][1]);
    mfma_settle(acc[4][1], acc[5][1], acc[6][1], acc[7][1]);
    STAMP0(s, 2);
    partial_put(TR, acc, 1, w, lane);
    __syncthreads();
    partial_store(rs, pout, TR, 1, w, lane);
    const unsigned nk = ld4(rs, cnt_off(K_KM, c, h));
    DRAIN();
    have_km = __builtin_amdgcn_readfirstlane(nk);
    __syncthreads();
    if (tid == 0) st4(rs, cnt_off(K_ATT, c, pp), (unsigned)(s + 1));
    STAMP0(s, 3);
  }
  TICKDUMP;
}

__device__ __forceinline__ float gelu_tanh(float x) {
  const float z = x * fmaf(x * x, -2.0f * 0.7978845608028654f * 0.044715f * 1.4426950408889634f, -2.0f * 0.7978845608028654f * 1.4426950408889634f);
  return x * __builtin_amdgcn_rcpf(1.0f + __builtin_amdgcn_exp2f(z));
}
__device__ __forceinline__ unsigned geglu2(float g0, float g1, float u0, float u1) {
  const unsigned gp = pack2(g0, g1), up = pack2(u0, u1);
  const unsigned ap = pack2(gelu_tanh(bflo(gp)), gelu_tanh(bfhi(gp)));
  return pack2(bflo(ap) * bflo(up), bfhi(ap) * bfhi(up));
}
__device__ __forceinline__ void role_ffn(const Params& p, rsrc_t rs, LAS unsigned char* L, int c, int sl) {
  const int tid = threadIdx.x, lane = tid & 63, w = __builtin_amdgcn_readfirstlane(tid >> 6);
  const int g = lane >> 4, r = lane & 15;
  v4u wg[2][16], wu[2][16], wd[8][4];
  stream_halves<4>(L, DFF, w, lane,
      [&](int m) { return (m == 0 ? p.wg : p.wu) + sl * 128; },
      [&](int i, LAS unsigned char* img) {
#pragma unroll
        for (int ks = 0; ks < 16; ++ks) {
          const v4u f = half_frag(img, w, ks, lane);
          if (i < 2) wg[i & 1][ks] = f; else wu[i & 1][ks] = f;
        }
      });
  wave_panel(p.wd + (size_t)sl * 128 * DM + w * 128, DM, L + w * PSTG, lane, [&](int t, int ks, v4u f) { wd[t][ks] = f; });
  __syncthreads();
  LAS unsigned char* X = L;
  LAS unsigned char* EX = L + IMG;
  LAS unsigned char* TR = L + IMG + 8192;
  const int fb = r * RSTR + g * 16;
  volatile LAS unsigned* RDY = (volatile LAS unsigned*)(L + 2 * IMG + 8192);
  const unsigned xbase = (unsigned)(OFF_XF + (size_t)c * 2 * MB);
  bool staged = false;
  TICKDECL;
  for (int s = 0; s < NSEG; ++s) {
    if (!staged) {
      wait_cnt<POLL>(rs, cnt_off(K_X1, c, 8 * w), 8, sl & 7, (unsigned)(s + 1), lane);
      STAMP0(s, 0);
      v4u v[8];
      rows_load(rs, xbase + (unsigned)s * 32768u, v, w, lane);
      rows_put(X, v, w, lane);
      __syncthreads();
    }
    STAMP0(s, 1);
    f32x4 ag[2][2], au[2][2];
    {
      v4u xf[4];
#pragma unroll
      for (int i = 0; i < 4; ++i) xf[i] = row_frag(X, 0, i, fb);
      SB();
      unsigned hv[4];
#pragma unroll
      for (int i = 0; i < 32; ++i) {
        const int ks = i & 15, tt = i >> 4;
        const v4u x = xf[i & 3];
        MFA(ks == 0, ag[0][tt], wg[0][ks], x);
        MFA(ks == 0, ag[1][tt], wg[1][ks], x);
        SB();
        if (i >= 1 && i + 3 < 32) xf[(i + 3) & 3] = row_frag(X, (i + 3) >> 4, (i + 3) & 15, fb);
        MFA(ks == 0, au[0][tt], wu[0][ks], x);
        MFA(ks == 0, au[1][tt], wu[1][ks], x);
        SB();
        if (i >= 19 && i < 27 && (i & 1)) {
          const int q = (i - 19) >> 1, t = q >> 1, j = (q & 1) * 2;
          hv[q] = geglu2(ag[t][0][j], ag[t][0][j + 1], au[t][0][j], au[t][0][j + 1]);
          SB();
        }
        if (i == 27) { *(LAS v4u*)(EX + w * 1024 + lane * 16) = v4u{hv[0], hv[1], hv[2], hv[3]}; SB(); }
      }
    }
    STAMP0(s, 4);
    __syncthreads();
    STAMP0(s, 5);
    const unsigned pout = (unsigned)(OFF_PF + (size_t)(s * NFFN + sl) * 32 * KB);
    f32x4 acc[8][2];
    unsigned nx = 0;
    {
      v4u hf[4];
#pragma unroll
      for (int i = 0; i < 4; ++i) hf[i] = *(const LAS v4u*)(EX + i * 1024 + lane * 16);
      mfma_settle(ag[0][1], ag[1][1], au[0][1], au[1][1]);
      SB();
      unsigned hv[4];
#pragma unroll
      for (int ks = 0; ks < 4; ++ks) {
        const int t = ks >> 1, j = (ks & 1) * 2;
        const unsigned gp = pack2(ag[t][1][j], ag[t][1][j + 1]), up = pack2(au[t][1][j], au[t][1][j + 1]);
        MFV(ks == 0, acc[0][0], wd[0][ks], hf[ks]); MFV(ks == 0, acc[1][0], wd[1][ks], hf[ks]);
        SB();
        const float g0 = gelu_tanh(bflo(gp));
        MFV(ks == 0, acc[2][0], wd[2][ks], hf[ks]); MFV(ks == 0, acc[3][0], wd[3][ks], hf[ks]);
        SB();
        const float g1 = gelu_tanh(bfhi(gp));
        MFV(ks == 0, acc[4][0], wd[4][ks], hf[ks]); MFV(ks == 0, acc[5][0], wd[5][ks], hf[ks]);
        SB();
        const unsigned ap = pack2(g0, g1);
        hv[ks] = pack2(bflo(ap) * bflo(up), bfhi(ap) * bfhi(up));
        MFV(ks == 0, acc[6][0], wd[6][ks], hf[ks]); MFV(ks == 0, acc[7][0], wd[7][ks], hf[ks]);
        SB();
      }
      *(LAS v4u*)(EX + (4 + w) * 1024 + lane * 16) = v4u{hv[0], hv[1], hv[2], hv[3]};
      mfma_settle(acc[0][0], acc[1][0], acc[2][0], acc[3][0]);
      mfma_settle(acc[4][0], acc[5][0], acc[6][0], acc[7][0]);
      partial_put(TR, acc, 0, w, lane);
      if (w == 0) nx = ld4(rs, cnt_off(K_X1, c, 0) + (unsigned)(lane & 31) * 128u);
      STAMP0(s, 6);
      __syncthreads();
      STAMP0(s, 7);
#pragma unroll
      for (int i = 0; i < 4; ++i) hf[i] = *(const LAS v4u*)(EX + (4 + i) * 1024 + lane * 16);
      v4u rw = partial_row(TR, 0, 0, w, lane);
      SB();
#pragma unroll
      for (int ks = 0; ks < 4; ++ks) {
#pragma unroll
        for (int dt = 0; dt < 4; ++dt) MFV(ks == 0, acc[dt][1], wd[dt][ks], hf[ks]);
        SB();
        partial_row_store(rs, pout, rw, 0, ks, w, lane);
        MFV(ks == 0, acc[4][1], wd[4][ks], hf[ks]); MFV(ks == 0, acc[5][1], wd[5][ks], hf[ks]);
        SB();
        if (ks < 3) rw = partial_row(TR, 0, ks + 1, w, lane);
        MFV(ks == 0, acc[6][1], wd[6][ks], hf[ks]); MFV(ks == 0, acc[7][1], wd[7][ks], hf[ks]);
        SB();
      }
    }
    mfma_settle(acc[0][1], acc[1][1], acc[2][1], acc[3][1]);
    mfma_settle(acc[4][1], acc[5][1], acc[6][1], acc[7][1]);
    STAMP0(s, 2);
    partial_put(TR, acc, 1, w, lane);
    if (w == 0) {
      const bool ok = __all(nx >= (unsigned)(s + 2)) && s + 1 < NSEG;
      if (lane == 0) RDY[0] = ok ? 1u : 0u;
    }
    __syncthreads();
    staged = RDY[0] != 0u;
    if (staged) {
      v4u nv[8];
      partial_store(rs, pout, TR, 1, w, lane);
      rows_load(rs, xbase + (unsigned)(s + 1) * 32768u, nv, w, lane);
      rows_put(X, nv, w, lane);
      DRAIN();
      __syncthreads();
      if (tid == 0) st4(rs, cnt_off(K_FFN, c, sl), (unsigned)(s + 1));
    } else {
      partial_store(rs, pout, TR, 1, w, lane);
      DRAIN();
      __syncthreads();
      if (tid == 0) st4(rs, cnt_off(K_FFN, c, sl), (unsigned)(s + 1));
    }
    STAMP0(s, 3);
  }
  TICKDUMP;
}

template <int NP>
__device__ __forceinline__ void sum_rows(rsrc_t rs, unsigned off, float (&a)[8]) {
  v4u v[NP];
#pragma unroll
  for (int i = 0; i < NP; ++i) v[i] = ld16(rs, off + (unsigned)i * 32768u);
  SB();
#pragma unroll
  for (int j = 0; j < 8; ++j) a[j] = 0.f;
  unsigned c0 = 0x00003f80u, c1 = 0x3f800000u;
  asm("" : "+v"(c0), "+v"(c1));
  const bf16x2 e0 = __builtin_bit_cast(bf16x2, c0), e1 = __builtin_bit_cast(bf16x2, c1);
#pragma unroll
  for (int i = 0; i < NP; ++i) {
#pragma unroll
    for (int q = 0; q < 4; ++q) {
      const unsigned xu = v[i][q];
      const bf16x2 x = __builtin_bit_cast(bf16x2, xu);
      a[2 * q] = __builtin_amdgcn_fdot2_f32_bf16(x, e0, a[2 * q], false);
      a[2 * q + 1] = __builtin_amdgcn_fdot2_f32_bf16(x, e1, a[2 * q + 1], false);
    }
  }
}
__device__ __forceinline__ void role_red(const Params& p, rsrc_t rs, LAS unsigned char* L, int t) {
  const int tid = threadIdx.x, lane = tid & 63, w = __builtin_amdgcn_readfirstlane(tid >> 6);
  const int c = w >> 1, ffn = w & 1;
  for (int s = 0; s < NSEG; ++s) {
    float a[8];
    if (!ffn) {
      wait_cnt<POLL>(rs, cnt_off(K_ATT, c, 0), NATT, t & 15, (unsigned)(s + 1), lane);
      STAMP(s, 2 * w);
      sum_rows<NATT>(rs, (unsigned)(OFF_PO + (size_t)s * NATT * 32 * KB) + (unsigned)(t * 1024 + lane * 16), a);
    } else {
      wait_cnt<POLL>(rs, cnt_off(K_FFN, c, 0), NFFN, t % NFFN, (unsigned)(s + 1), lane);
      STAMP(s, 2 * w);
      sum_rows<NFFN>(rs, (unsigned)(OFF_PF + (size_t)s * NFFN * 32 * KB) + (unsigned)(t * 1024 + lane * 16), a);
    }
    v4u xo;
    if (w == 0) xo = *(const v4u*)(p.x + (size_t)(s * SEG + t) * DM + lane * 8);
    else xo = *(volatile LAS v4u*)(L + s * 1024 + lane * 16);
    float x[8] = {bflo(xo.x), bfhi(xo.x), bflo(xo.y), bfhi(xo.y), bflo(xo.z), bfhi(xo.z), bflo(xo.w), bfhi(xo.w)};
#pragma unroll
    for (int j = 0; j < 8; ++j) x[j] = bf16r(x[j] + bf16r(a[j]));
    v4u xn; xn.x = pack2(x[0], x[1]); xn.y = pack2(x[2], x[3]); xn.z = pack2(x[4], x[5]); xn.w = pack2(x[6], x[7]);
    if (w == 3) {
      *(v4u*)(p.out + (size_t)(s * SEG + t) * DM + lane * 8) = xn;
    } else {
      *(volatile LAS v4u*)(L + s * 1024 + lane * 16) = xn;
      float y[8];
      layernorm8(x, y);
      v4u o; o.x = pack2(y[0], y[1]); o.y = pack2(y[2], y[3]); o.z = pack2(y[4], y[5]); o.w = pack2(y[6], y[7]);
      const unsigned dst = (ffn ? (unsigned)(OFF_XA + 2 * MB) : (unsigned)(OFF_XF + (size_t)c * 2 * MB)) + (unsigned)((s * SEG + t) * 1024 + lane * 16);
      st16(rs, dst, o);
      LDSWAIT();
      DRAIN();
      const unsigned f = ffn ? cnt_off(K_X2, 0, t) : cnt_off(K_X1, c, t);
      if (lane == 0) st4(rs, f, (unsigned)(s + 1));
    }
    STAMP(s, 2 * w + 1);
  }
}

__global__ void __launch_bounds__(256, 1) k_main(Params p) {
  __shared__ __attribute__((aligned(16))) unsigned char lds[LDS_BYTES];
  LAS unsigned char* L = (LAS unsigned char*)lds;
  const rsrc_t rs = __builtin_amdgcn_make_buffer_rsrc((void*)p.ws, 0, (int)WS_BYTES, 0x00020000);
  const int b = blockIdx.x;
  if (b >= 192 && b < 220)
    for (int i = 0; i < LATE; ++i) __builtin_amdgcn_s_sleep(100);
  if (b < 64) role_chain(p, rs, L, b >> 5, b & 31);
  else if (b < 96) role_creduce(p, rs, L, (b - 64) >> 4, (b - 64) & 15);
  else if (b < 160) role_qkv(p, rs, L, (b - 96) >> 5, (b - 96) & 31);
  else if (b < 192) role_att(p, rs, L, (b - 160) >> 4, (b - 160) & 15);
  else if (b < 220) role_ffn(p, rs, L, (b - 192) / NFFN, (b - 192) % NFFN);
  else if (b < 252) role_red(p, rs, L, b - 220);
}

extern "C" void kernel_launch(void* const* d_in, const int* in_sizes, int n_in,
                              void* d_out, int out_size, void* d_ws, size_t ws_size,
                              hipStream_t stream) {
  PrepParams pp;
  pp.x = (const u16*)d_in[0];
  pp.wq = (const u16*)d_in[2];
  pp.wk = (const u16*)d_in[3];
  pp.wv = (const u16*)d_in[4];
  pp.wo = (const u16*)d_in[5];
  pp.ws = (unsigned char*)d_ws;
  k_prep<<<NJ_ALL, 256, 0, stream>>>(pp);
  Params p;
  p.x = (const u16*)d_in[0];
  p.mem0 = (const u16*)d_in[1];
  p.wg = (const u16*)d_in[8];
  p.wu = (const u16*)d_in[9];
  p.wd = (const u16*)d_in[10];
  p.out = (u16*)d_out;
  p.ws = (unsigned char*)d_ws;
  k_main<<<252, 256, 0, stream>>>(p);
}
```

```cpp
#include <hip/hip_runtime.h>
#include <hip/hip_bf16.h>
#include <stdint.h>

#define GAS __attribute__((address_space(1)))
#define LAS __attribute__((address_space(3)))
typedef unsigned v4u __attribute__((ext_vector_type(4)));
typedef unsigned v2u __attribute__((ext_vector_type(2)));
typedef float f32x4 __attribute__((ext_vector_type(4)));
typedef float f32x2 __attribute__((ext_vector_type(2)));
typedef short bf16x8 __attribute__((ext_vector_type(8)));
typedef __bf16 bf16x2 __attribute__((ext_vector_type(2)));
typedef GAS unsigned gu32;
typedef unsigned short u16;
#define RLX_AGENT __ATOMIC_RELAXED, __HIP_MEMORY_SCOPE_AGENT
#define SB() __builtin_amdgcn_sched_barrier(0)
#define DRAIN() asm volatile("s_waitcnt vmcnt(0)" ::: "memory")
#define LDSWAIT() asm volatile("s_waitcnt lgkmcnt(0)" ::: "memory")

constexpr int S_TOK = 2048, DM = 512, NH = 32, DH = 128, NMEM = 8, SEG = 32, NSEG = 64, DFF = 1792;
constexpr int NFFN = 14, NATT = 16;

constexpr size_t KB = 1024, MB = 1024 * 1024;
constexpr size_t OFF_ABORT = 0;
enum { K_QKV = 0, K_KM = 1, K_ATT = 2, K_X1 = 3, K_FFN = 4, K_X2 = 5, K_COL = 6 };
constexpr size_t OFF_CNT = 4 * KB;
__host__ __device__ constexpr unsigned cnt_off(int kind, int c, int idx) { return (unsigned)(OFF_CNT + (size_t)((kind * 2 + c) * 32 + idx) * 128); }
constexpr size_t ZERO_BYTES = 64 * KB;
constexpr size_t ROW = 32 * KB;
constexpr size_t OFF_WQKV = 2 * MB;
constexpr size_t OFF_WO = OFF_WQKV + 12 * MB;
constexpr size_t OFF_WG = OFF_WO + 4 * MB;
constexpr size_t OFF_WU = OFF_WG + NFFN * 128 * KB;
constexpr size_t OFF_WD = OFF_WU + NFFN * 128 * KB;
constexpr size_t OFF_XA = 24 * MB;
constexpr size_t OFF_XF = OFF_XA + 4 * MB;
constexpr size_t OFF_KVM = OFF_XF + 4 * MB;
constexpr size_t OFF_QKVF = OFF_KVM + 16 * MB;
constexpr size_t OFF_PO = OFF_QKVF + 96 * MB;
constexpr size_t OFF_PF = OFF_PO + 32 * MB;
constexpr size_t OFF_P = OFF_PF + 28 * MB;
constexpr size_t CR_BLK = 16 * ROW, P_CHAIN = 16 * CR_BLK, M_ROW0 = 8 * ROW, M_SLOT = 2 * ROW;
constexpr size_t WS_BYTES = OFF_P + 2 * P_CHAIN + CR_BLK;
__device__ __forceinline__ unsigned p_region(const unsigned char* ws, int c) {
  return (unsigned)OFF_P + (unsigned)((0u - (unsigned)(uintptr_t)ws - (unsigned)OFF_P) & (unsigned)(CR_BLK - 1)) + (unsigned)c * (unsigned)P_CHAIN;
}
static_assert(OFF_CNT + 14 * 32 * 128 <= ZERO_BYTES, "flags");
static_assert(OFF_WD + NFFN * 128 * KB <= OFF_XA, "weights");
static_assert(WS_BYTES <= 256 * MB, "workspace");

constexpr unsigned kSpinCap = 1u << 17;
constexpr int POLL = 3;
constexpr int CAL_N = 2;
constexpr int LATE = 2;
constexpr int CR_SLEEP = 56;
constexpr int LDS_BYTES = 163840;

struct Params {
  const u16* x;
  const u16* mem0;
  const u16* wg;
  const u16* wu;
  const u16* wd;
  u16* out;
  unsigned char* ws;
};
struct PrepParams {
  const u16* x;
  const u16* wq;
  const u16* wk;
  const u16* wv;
  const u16* wo;
  unsigned char* ws;
};

__device__ __forceinline__ unsigned xcc_id() { return (unsigned)__builtin_amdgcn_s_getreg(20 | (3 << 11)) & 7u; }
__device__ __forceinline__ unsigned pack2(float a, float b) {
  f32x2 v = {a, b};
  bf16x2 r = __builtin_convertvector(v, bf16x2);
  return __builtin_bit_cast(unsigned, r);
}
__device__ __forceinline__ float bf16r(float a) { return __uint_as_float(pack2(a, 0.f) << 16); }
__device__ __forceinline__ float bflo(unsigned u) { return __uint_as_float(u << 16); }
__device__ __forceinline__ float bfhi(unsigned u) { return __uint_as_float(u & 0xffff0000u); }
#define MFMA "v_mfma_f32_16x16x32_bf16 "
template <bool FIRST> __device__ __forceinline__ void mfa(f32x4& c, v4u a, v4u b) {
  if (FIRST) asm(MFMA "%0, %1, %2, 0" : "=&v"(c) : "a"(a), "v"(b));
  else asm(MFMA "%0, %1, %2, %0" : "+v"(c) : "a"(a), "v"(b));
}
template <bool FIRST> __device__ __forceinline__ void mfv(f32x4& c, v4u a, v4u b) {
  if (FIRST) asm(MFMA "%0, %1, %2, 0" : "=&v"(c) : "v"(a), "v"(b));
  else asm(MFMA "%0, %1, %2, %0" : "+v"(c) : "v"(a), "v"(b));
}
#define MFA(first, c, a, b) do { if (first) mfa<true>(c, a, b); else mfa<false>(c, a, b); } while (0)
#define MFV(first, c, a, b) do { if (first) mfv<true>(c, a, b); else mfv<false>(c, a, b); } while (0)
__device__ __forceinline__ void mfma_pv(f32x4& c0, f32x4& c1, v4u a0, v4u a1, v4u m, v4u p0, v4u pl, v4u ph) {
  asm("s_nop 1\n\t"
      MFMA "%0, %2, %5, 0\n\t"
      MFMA "%1, %3, %5, 0\n\t"
      MFMA "%0, %4, %6, %0\n\t"
      MFMA "%1, %4, %7, %1"
      : "=&v"(c0), "=&v"(c1)
      : "v"(a0), "v"(a1), "v"(m), "v"(p0), "v"(pl), "v"(ph));
}
__device__ __forceinline__ void mfma_k2(f32x4& c, v4u a, v4u b0, v4u b1) {
  asm("s_nop 1\n\t"
      MFMA "%0, %1, %2, 0\n\t"
      MFMA "%0, %1, %3, %0"
      : "=&v"(c)
      : "v"(a), "v"(b0), "v"(b1));
}
__device__ __forceinline__ void mfma_settle2(f32x4& c0, f32x4& c1) { asm("s_nop 7\n\ts_nop 1" : "+v"(c0), "+v"(c1)); }
__device__ __forceinline__ void mfma_settle3(f32x4& c0, f32x4& c1, f32x4& c2) { asm("s_nop 7\n\ts_nop 1" : "+v"(c0), "+v"(c1), "+v"(c2)); }
__device__ __forceinline__ void mfma_settle(f32x4& c0, f32x4& c1, f32x4& c2, f32x4& c3) {
  asm("s_nop 7\n\ts_nop 1" : "+v"(c0), "+v"(c1), "+v"(c2), "+v"(c3));
}
__device__ __forceinline__ v4u pack8(f32x4 a, f32x4 b) {
  v4u r;
  r.x = pack2(a.x, a.y); r.y = pack2(a.z, a.w); r.z = pack2(b.x, b.y); r.w = pack2(b.z, b.w);
  return r;
}
__device__ __forceinline__ float xmax(float x) {
  auto s = __builtin_amdgcn_permlane16_swap(__float_as_uint(x), __float_as_uint(x), false, false);
  x = fmaxf(__uint_as_float(s[0]), __uint_as_float(s[1]));
  auto t = __builtin_amdgcn_permlane32_swap(__float_as_uint(x), __float_as_uint(x), false, false);
  return fmaxf(__uint_as_float(t[0]), __uint_as_float(t[1]));
}
__device__ __forceinline__ float xsum(float x) {
  auto s = __builtin_amdgcn_permlane16_swap(__float_as_uint(x), __float_as_uint(x), false, false);
  x = __uint_as_float(s[0]) + __uint_as_float(s[1]);
  auto t = __builtin_amdgcn_permlane32_swap(__float_as_uint(x), __float_as_uint(x), false, false);
  return __uint_as_float(t[0]) + __uint_as_float(t[1]);
}
__device__ __forceinline__ unsigned ror8(unsigned x) {
  return (unsigned)__builtin_amdgcn_update_dpp(0, (int)x, 0x128, 0xf, 0xf, false);
}
__device__ __forceinline__ float ror8f(float x) { return __uint_as_float(ror8(__float_as_uint(x))); }
template <int CTRL> __device__ __forceinline__ float dppf(float x) {
  return __uint_as_float((unsigned)__builtin_amdgcn_update_dpp(0, (int)__float_as_uint(x), CTRL, 0xf, 0xf, false));
}
__device__ __forceinline__ float wave_sum(float x) {
  x += dppf<0x121>(x); x += dppf<0x122>(x); x += dppf<0x124>(x); x += dppf<0x128>(x);
  return xsum(x);
}
typedef __amdgpu_buffer_rsrc_t rsrc_t;
__device__ __forceinline__ v4u ld16(rsrc_t rs, unsigned off) { return __builtin_amdgcn_raw_buffer_load_b128(rs, (int)off, 0, 16); }
__device__ __forceinline__ v4u ld16s(rsrc_t rs, unsigned voff, unsigned soff) { return __builtin_amdgcn_raw_buffer_load_b128(rs, (int)voff, (int)soff, 16); }
__device__ __forceinline__ v2u ld8(rsrc_t rs, unsigned off) { return __builtin_amdgcn_raw_buffer_load_b64(rs, (int)off, 0, 16); }
__device__ __forceinline__ v2u ld8s(rsrc_t rs, unsigned voff, unsigned soff) { return __builtin_amdgcn_raw_buffer_load_b64(rs, (int)voff, (int)soff, 16); }
__device__ __forceinline__ unsigned ld4(rsrc_t rs, unsigned off) { return __builtin_amdgcn_raw_buffer_load_b32(rs, (int)off, 0, 16); }
__device__ __forceinline__ v4u ld16p(rsrc_t rs, unsigned off) { return __builtin_amdgcn_raw_buffer_load_b128(rs, (int)off, 0, 0); }
__device__ __forceinline__ void st16(rsrc_t rs, unsigned off, v4u v) { __builtin_amdgcn_raw_buffer_store_b128(v, rs, (int)off, 0, 16); }
__device__ __forceinline__ void st16s(rsrc_t rs, unsigned voff, unsigned soff, v4u v) { __builtin_amdgcn_raw_buffer_store_b128(v, rs, (int)voff, (int)soff, 16); }
__device__ __forceinline__ void st8(rsrc_t rs, unsigned off, v2u v) { __builtin_amdgcn_raw_buffer_store_b64(v, rs, (int)off, 0, 16); }
__device__ __forceinline__ void st4(rsrc_t rs, unsigned off, unsigned v) { __builtin_amdgcn_raw_buffer_store_b32(v, rs, (int)off, 0, 16); }

__device__ __forceinline__ bool spin_fail(unsigned& spins, rsrc_t rs) {
  ++spins;
  if (spins > kSpinCap) { st4(rs, (unsigned)OFF_ABORT, 1u); return true; }
  if ((spins & 127u) == 0u) return __builtin_amdgcn_readfirstlane(ld4(rs, (unsigned)OFF_ABORT)) != 0u;
  return false;
}
template <int SLEEP>
__device__ __forceinline__ void wait_cnt(rsrc_t rs, unsigned off, int n, int probe, unsigned want, int lane) {
  unsigned spins = 0;
  for (;;) {
    const unsigned v = ld4(rs, off + (unsigned)probe * 128u);
    if (__builtin_amdgcn_readfirstlane(v) >= want) break;
    if (spin_fail(spins, rs)) break;
    __builtin_amdgcn_s_sleep(SLEEP);
  }
  if (n > 1) {
    for (;;) {
      const unsigned v = ld4(rs, off + (unsigned)(lane < n ? lane : 0) * 128u);
      if (__all(v >= want)) break;
      if (spin_fail(spins, rs)) break;
      __builtin_amdgcn_s_sleep(2);
    }
  }
  __builtin_amdgcn_fence(__ATOMIC_ACQUIRE, "wavefront");
}

__device__ __forceinline__ unsigned wait_table(rsrc_t rs, int c, int lane) {
  unsigned spins = 0, v;
  for (;;) {
    v = ld4(rs, cnt_off(K_COL, c, 0) + (unsigned)(lane & 15) * 128u);
    if (__all(v != 0u)) break;
    if (spin_fail(spins, rs)) break;
    __builtin_amdgcn_s_sleep(8);
  }
  return v - 1u;
}
__device__ __forceinline__ unsigned mem_copy(unsigned tab, unsigned x) {
  const unsigned long long m = __ballot(((tab >> 3) & 7u) == x) & 0xffffull;
  const int own = m == 0ull ? 0 : __builtin_ctzll(m);
  return (unsigned)own * (unsigned)CR_BLK + (unsigned)M_ROW0 + ((unsigned)__builtin_amdgcn_readlane(tab, own) & 7u) * 4096u;
}

__device__ __forceinline__ void layernorm8(const float (&x)[8], float (&y)[8]) {
  float s = 0.f;
#pragma unroll
  for (int j = 0; j < 8; ++j) s += x[j];
  const float mean = wave_sum(s) * (1.0f / DM);
  float q = 0.f;
#pragma unroll
  for (int j = 0; j < 8; ++j) { const float d = x[j] - mean; q += d * d; }
  const float var = wave_sum(q) * (1.0f / DM);
  const float m = bf16r(mean);
  const float rden = 1.0f / bf16r(sqrtf(bf16r(bf16r(var) + 1e-5f)));
#pragma unroll
  for (int j = 0; j < 8; ++j) y[j] = bf16r(x[j] - m) * rden;
}

typedef short v4s __attribute__((ext_vector_type(4)));
__device__ __forceinline__ v2u tr8(const LAS unsigned char* a) {
  return __builtin_bit_cast(v2u, __builtin_amdgcn_ds_read_tr16_b64_v4i16((LAS v4s*)a));
}

constexpr int NJ_QKV = 3 * 16 * 16;
constexpr int NJ_WO = 32 * 4 * 2;
constexpr int NJ_LN = S_TOK / 4;
constexpr int NJ_INIT = 256;
constexpr int NJ_ALL = NJ_QKV + NJ_WO + NJ_LN + NJ_INIT;

__global__ void __launch_bounds__(256) k_prep(PrepParams p) {
  __shared__ __attribute__((aligned(16))) unsigned char tile[32 * 528];
  const int tid = threadIdx.x, lane = tid & 63, w = tid >> 6;
  int job = blockIdx.x;
  if (job < NJ_QKV + NJ_WO) {
    const bool pi = job >= NJ_QKV;
    const u16* src; int ld; size_t dst, tstride;
    if (!pi) {
      const int m = job >> 8, ks = (job >> 4) & 15, hp = job & 15;
      src = (m == 0 ? p.wq : (m == 1 ? p.wk : p.wv)) + (size_t)(32 * ks) * (NH * DH) + hp * 256; ld = NH * DH;
      dst = OFF_WQKV + ((size_t)(m * 32 + 2 * hp) * 8 * 16 + ks) * KB; tstride = 16 * KB;
    } else {
      job -= NJ_QKV;
      const int h = job >> 3, ks = (job >> 1) & 3, nb = job & 1;
      src = p.wo + (size_t)(h * DH + 32 * ks) * DM + nb * 256; ld = DM;
      dst = OFF_WO + ((size_t)(h * 32 + nb * 16) * 4 + ks) * KB; tstride = 4 * KB;
    }
    v4u v[4];
#pragma unroll
    for (int it = 0; it < 4; ++it) v[it] = *(const v4u*)(src + (size_t)(it * 8 + (tid >> 5)) * ld + (tid & 31) * 8);
#pragma unroll
    for (int it = 0; it < 4; ++it) *(v4u*)(tile + (it * 8 + (tid >> 5)) * 528 + (tid & 31) * 16) = v[it];
    __syncthreads();
    const int g = lane >> 4, q = (lane >> 2) & 3, pc = lane & 3;
    const LAS unsigned char* T = (const LAS unsigned char*)tile;
#pragma unroll
    for (int it = 0; it < 4; ++it) {
      const int t = w * 4 + it;
      const LAS unsigned char* a = pi ? T + (4 * g + q) * 528 + (t >> 1) * 64 + pc * 16 + (t & 1) * 8 : T + (8 * g + q) * 528 + t * 32 + pc * 8;
      const v2u lo = tr8(a), hi = tr8(a + (pi ? 16 : 4) * 528);
      *(v4u*)(p.ws + dst + (size_t)t * tstride + lane * 16) = v4u{lo.x, lo.y, hi.x, hi.y};
    }
    return;
  }
  job -= NJ_QKV + NJ_WO;
  if (job < NJ_LN) {
    const int row = job * 4 + w;
    const v4u v = *(const v4u*)(p.x + (size_t)row * DM + lane * 8);
    float x[8] = {bflo(v.x), bfhi(v.x), bflo(v.y), bfhi(v.y), bflo(v.z), bfhi(v.z), bflo(v.w), bfhi(v.w)}, y[8];
    layernorm8(x, y);
    v4u o; o.x = pack2(y[0], y[1]); o.y = pack2(y[2], y[3]); o.z = pack2(y[4], y[5]); o.w = pack2(y[6], y[7]);
    *(v4u*)(p.ws + OFF_XA + (size_t)row * 1024 + lane * 16) = o;
    return;
  }
  job -= NJ_LN;
  {
    const v4u z = {0u, 0u, 0u, 0u};
    for (size_t i = (size_t)job * 256 + tid; i < ZERO_BYTES / 16; i += (size_t)NJ_INIT * 256) *(v4u*)(p.ws + i * 16) = z;
    *(v4u*)(p.ws + p_region(p.ws, 0) + (size_t)(job >> 3) * CR_BLK + (job & 7) * 4096 + tid * 16) = z;
  }
}

__device__ __forceinline__ int hkey(int row) { return ((row >> 1) & 1) | ((row >> 2) & 2); }
__device__ __forceinline__ void half_issue(const u16* src, int ld, int t, LAS unsigned char* img, int w, int lane) {
  const int row = lane >> 3, pos = (lane >> 1) & 3;
#pragma unroll
  for (int i = 0; i < 16; ++i) {
    const int r0 = (4 * i + w) * 8 + row;
    const u16* q = src + (size_t)r0 * ld + (2 * (pos ^ hkey(r0)) + t) * 16 + (lane & 1) * 8;
    const unsigned dst = (unsigned)(uintptr_t)img + (unsigned)((4 * i + w) * 1024);
    unsigned keep;
    asm volatile("s_mov_b32 %0, m0\n\ts_mov_b32 m0, %2\n\ts_nop 0\n\tglobal_load_lds_dwordx4 %1, off\n\ts_mov_b32 m0, %0"
                 : "=&s"(keep) : "v"(q), "s"(dst) : "memory");
  }
  SB();
}
__device__ __forceinline__ v4u half_frag(const LAS unsigned char* img, int w, int ks, int lane) {
  const int row = 8 * (lane >> 4) + ((lane >> 2) & 3);
  const LAS unsigned char* a = img + (32 * ks + row) * 128 + ((w ^ hkey(row)) * 32) + (lane & 3) * 8;
  const v2u lo = tr8(a), hi = tr8(a + 512);
  return v4u{lo.x, lo.y, hi.x, hi.y};
}
#define RAWBAR() do { asm volatile("s_waitcnt lgkmcnt(0)" ::: "memory"); __builtin_amdgcn_s_barrier(); asm volatile("" ::: "memory"); } while (0)
template <int NH2, class SRC, class USE>
__device__ __forceinline__ void stream_halves(LAS unsigned char* buf, int ld, int w, int lane, SRC&& src, USE&& use) {
  half_issue(src(0), ld, 0, buf, w, lane);
  half_issue(src(0), ld, 1, buf + 65536, w, lane);
#pragma unroll
  for (int i = 0; i < NH2; ++i) {
    if (i + 1 < NH2) asm volatile("s_waitcnt vmcnt(16)" ::: "memory");
    else asm volatile("s_waitcnt vmcnt(0)" ::: "memory");
    RAWBAR();
    use(i, buf + (i & 1) * 65536);
    RAWBAR();
    if (i + 2 < NH2) half_issue(src((i + 2) >> 1), ld, i & 1, buf + (i & 1) * 65536, w, lane);
  }
}
constexpr int PSTG = 16 * 272;
template <class OUT>
__device__ __forceinline__ void wave_panel(const u16* src, int ld, LAS unsigned char* stg, int lane, OUT&& out) {
  const u16* q = src + (size_t)(lane >> 4) * ld + (lane & 15) * 8;
  const LAS unsigned char* a = stg + (4 * (lane >> 4) + ((lane >> 2) & 3)) * 272 + (lane & 3) * 16;
#pragma unroll
  for (int bt = 0; bt < 2; ++bt) {
    v4u v[16];
#pragma unroll
    for (int i = 0; i < 16; ++i) v[i] = *(const v4u*)(q + (size_t)(64 * bt + 4 * i) * ld);
    SB();
#pragma unroll
    for (int k2 = 0; k2 < 2; ++k2) {
      v2u lo[8];
#pragma unroll
      for (int hf = 0; hf < 2; ++hf) {
#pragma unroll
        for (int i = 0; i < 4; ++i) *(LAS v4u*)(stg + ((lane >> 4) + 4 * i) * 272 + (lane & 15) * 16) = v[8 * k2 + 4 * hf + i];
#pragma unroll
        for (int t = 0; t < 8; ++t) {
          const v2u x = tr8(a + (t >> 1) * 64 + (t & 1) * 8);
          if (hf == 0) lo[t] = x;
          else out(t, 2 * bt + k2, v4u{lo[t].x, lo[t].y, x.x, x.y});
        }
        LDSWAIT();
        SB();
      }
    }
  }
}

constexpr float kScale = 0.08838834764831845f;
constexpr float kNegBig = -3.0e38f;
constexpr float kLog2e = 1.4426950408889634f;

__device__ __forceinline__ void softmax_frags(f32x4 sx0, f32x4 sx1, f32x4 sm, unsigned ok0, unsigned ok1, bool okm, v4u& p0, v4u& p1) {
  unsigned u[6] = {pack2(sx0.x, sx0.y), pack2(sx0.z, sx0.w), pack2(sx1.x, sx1.y), pack2(sx1.z, sx1.w), pack2(sm.x, sm.y), pack2(sm.z, sm.w)};
  float a[12];
#pragma unroll
  for (int j = 0; j < 6; ++j) {
    u[j] = pack2(bflo(u[j]) * kScale, bfhi(u[j]) * kScale);
    a[2 * j] = bflo(u[j]); a[2 * j + 1] = bfhi(u[j]);
  }
#pragma unroll
  for (int r = 0; r < 4; ++r) {
    a[r] = ((ok0 >> r) & 1u) ? a[r] : kNegBig;
    a[4 + r] = ((ok1 >> r) & 1u) ? a[4 + r] : kNegBig;
    a[8 + r] = okm ? a[8 + r] : kNegBig;
  }
  float mx = fmaxf(fmaxf(fmaxf(a[0], a[1]), fmaxf(a[2], a[3])), fmaxf(fmaxf(a[4], a[5]), fmaxf(a[6], a[7])));
  mx = fmaxf(mx, fmaxf(fmaxf(a[8], a[9]), fmaxf(a[10], a[11])));
  mx = xmax(mx);
  const float nm = -mx * kLog2e;
#pragma unroll
  for (int j = 0; j < 12; ++j) a[j] = __builtin_amdgcn_exp2f(fmaf(a[j], kLog2e, nm));
  float sum = ((a[0] + a[1]) + (a[2] + a[3])) + ((a[4] + a[5]) + (a[6] + a[7])) + ((a[8] + a[9]) + (a[10] + a[11]));
  sum = xsum(sum);
  const float inv = __builtin_amdgcn_rcpf(sum);
  p0.x = pack2(a[0] * inv, a[1] * inv); p0.y = pack2(a[2] * inv, a[3] * inv);
  p0.z = pack2(a[4] * inv, a[5] * inv); p0.w = pack2(a[6] * inv, a[7] * inv);
  p1.x = pack2(a[8] * inv, a[9] * inv); p1.y = pack2(a[10] * inv, a[11] * inv);
  p1.z = 0u; p1.w = 0u;
}

__device__ __forceinline__ void rows_load(rsrc_t rs, unsigned goff, v4u (&v)[8], int w, int lane) {
#pragma unroll
  for (int i = 0; i < 8; ++i) v[i] = ld16s(rs, (unsigned)lane * 16u + (unsigned)(i & 3) * 1024u, goff + (unsigned)(8 * w + (i & 4)) * 1024u);
  SB();
}
constexpr int RSTR = 1040;
constexpr int IMG = 32 * RSTR;
__device__ __forceinline__ void rows_put(LAS unsigned char* L, const v4u (&v)[8], int w, int lane) {
#pragma unroll
  for (int i = 0; i < 8; ++i) *(LAS v4u*)(L + (8 * w + i) * RSTR + lane * 16) = v[i];
}
__device__ __forceinline__ v4u row_frag(const LAS unsigned char* L, int tt, int ks, int fb) {
  return *(const LAS v4u*)(L + fb + ks * 64 + tt * 16 * RSTR);
}
__device__ __forceinline__ void partial_put(LAS unsigned char* L, const f32x4 (&acc)[8][2], int tt, int w, int lane) {
  LAS unsigned char* B = L + (16 * tt + (lane & 15)) * RSTR + w * 256 + (lane >> 4) * 16;
#pragma unroll
  for (int m = 0; m < 4; ++m) *(LAS v4u*)(B + m * 64) = pack8(acc[2 * m][tt], acc[2 * m + 1][tt]);
}
__device__ __forceinline__ v4u partial_row(const LAS unsigned char* L, int tt, int k, int w, int lane) {
  return *(const LAS v4u*)(L + (16 * tt + 4 * w + k) * RSTR + lane * 16);
}
__device__ __forceinline__ void partial_row_store(rsrc_t rs, unsigned goff, v4u v, int tt, int k, int w, int lane) {
  st16s(rs, (unsigned)lane * 16u + (unsigned)k * 1024u, goff + (unsigned)(16 * tt + 4 * w) * 1024u, v);
}
__device__ __forceinline__ void partial_store(rsrc_t rs, unsigned goff, const LAS unsigned char* L, int tt, int w, int lane) {
  v4u v[4];
#pragma unroll
  for (int k = 0; k < 4; ++k) v[k] = partial_row(L, tt, k, w, lane);
#pragma unroll
  for (int k = 0; k < 4; ++k) partial_row_store(rs, goff, v[k], tt, k, w, lane);
  SB();
}

constexpr int CH_KV = 0, CH_MEM = 16384, CH_EX = CH_MEM + 8192, CH_WV = CH_EX + 6144, CH_FL = CH_WV + 131072 + 2032;
__device__ __forceinline__ void role_chain(const Params& p, rsrc_t rs, LAS unsigned char* L, int c, int h) {
  const int tid = threadIdx.x, lane = tid & 63, w = __builtin_amdgcn_readfirstlane(tid >> 6);
  const int g = lane >> 4, r = lane & 15, r8 = lane & 7;
  v4u wq[2][16], wk[2][16], wo[8][4];
#pragma unroll
  for (int t = 0; t < 2; ++t)
#pragma unroll
    for (int ks = 0; ks < 16; ++ks) {
      const unsigned o = (unsigned)(((h * 8 + 2 * w + t) * 16 + ((4 * w + ks) & 15)) * 1024 + lane * 16);
      wq[t][ks] = ld16p(rs, (unsigned)OFF_WQKV + o);
      wk[t][ks] = ld16p(rs, (unsigned)(OFF_WQKV + 4 * MB) + o);
    }
  for (int i = 0; i < 32; ++i) {
    const int t = i >> 4, j = i & 15;
    *(LAS v4u*)(L + CH_WV + ((2 * w + t) * 16 + j) * 1024 + lane * 16) =
        ld16p(rs, (unsigned)(OFF_WQKV + 8 * MB) + (unsigned)(((h * 8 + 2 * w + t) * 16 + ((4 * w + j) & 15)) * 1024 + lane * 16));
  }
#pragma unroll
  for (int dt = 0; dt < 8; ++dt)
#pragma unroll
    for (int ks = 0; ks < 4; ++ks) wo[dt][ks] = ld16p(rs, (unsigned)OFF_WO + (unsigned)(((h * 32 + 8 * w + dt) * 4 + ks) * 1024 + lane * 16));
  v4u mo[4];
#pragma unroll
  for (int j = 0; j < 4; ++j) {
    mo[j] = *(const v4u*)(p.mem0 + r8 * DM + (4 * w + j) * 32 + g * 8);
    if (r < 8) *(LAS v4u*)(L + CH_MEM + (4 * w + j) * 512 + g * 128 + r * 16) = mo[j];
  }
  const unsigned qkv_base = (unsigned)(OFF_QKVF + (size_t)c * NSEG * NH * 24 * KB) + (unsigned)h * 24u * 1024u + 8192u;
  const unsigned kvm_base = (unsigned)(OFF_KVM + (size_t)c * NSEG * NH * 4 * KB) + (unsigned)h * 4096u;
  {
    wait_cnt<8>(rs, cnt_off(K_QKV, c, h), 1, 0, 1u, lane);
    v4u v[4];
#pragma unroll
    for (int i = 0; i < 4; ++i) v[i] = ld16(rs, qkv_base + (unsigned)((4 * w + i) * 1024 + lane * 16));
    SB();
#pragma unroll
    for (int i = 0; i < 4; ++i) *(LAS v4u*)(L + CH_KV + (4 * w + i) * 1024 + lane * 16) = v[i];
  }
  unsigned p_off[2], m_base;
  {
    const unsigned tab = wait_table(rs, c, lane);
#pragma unroll
    for (int q = 0; q < 2; ++q) {
      const unsigned ca = (unsigned)__builtin_amdgcn_readlane(tab, 4 * w + 2 * q) & 7u, cb = (unsigned)__builtin_amdgcn_readlane(tab, 4 * w + 2 * q + 1) & 7u;
      p_off[q] = (unsigned)((4 * w + 2 * q + (r >> 3)) * CR_BLK + g * 2 * ROW + h * 128 + r8 * 16) + (r < 8 ? ca : cb) * 4096u;
    }
    m_base = p_region(p.ws, c) + mem_copy(tab, xcc_id()) + (unsigned)((w >> 1) * ROW + (w & 1) * 2048 + g * 128 + r8 * 16);
  }
  unsigned have_qkv = 1;
  volatile LAS v4u* FL = (volatile LAS v4u*)(L + CH_FL);
  if (tid == 0) *FL = v4u{0u, 0u, 0u, 0u};
  __syncthreads();
  v4u wv0[2], wv1[2];
#pragma unroll
  for (int j = 0; j < 2; ++j) {
    wv0[j] = *(const LAS v4u*)(L + CH_WV + 2 * w * 16384 + lane * 16 + j * 1024);
    wv1[j] = *(const LAS v4u*)(L + CH_WV + 2 * w * 16384 + 16384 + lane * 16 + j * 1024);
  }
  for (int s = 0; s < NSEG; ++s) {
    const LAS unsigned char* MF = L + CH_MEM + g * 128 + r8 * 16;
    const LAS unsigned char* WV = L + CH_WV + 2 * w * 16384 + lane * 16;
    f32x4 aq0, aq1, ak0, ak1, av0, av1;
    v4u kx[8];
    {
      v4u fl = *FL;
      v4u mf[4];
      SB();
#define QKV_STEP(j, m) do { \
        MFA((j) == 0, aq0, wq[0][j], m); MFA((j) == 0, aq1, wq[1][j], m); SB(); \
        if ((j) >= 4 && (j) + 3 < 16) mf[((j) + 3) & 3] = *(const LAS v4u*)(MF + ((w + (((j) + 3) >> 2)) & 3) * 2048 + (((j) + 3) & 3) * 512); \
        MFA((j) == 0, ak0, wk[0][j], m); MFA((j) == 0, ak1, wk[1][j], m); SB(); \
        if ((j) >= 12) kx[(((j) - 12) & 1) + 4 * (((j) - 12) >> 1)] = *(const LAS v4u*)(L + CH_KV + ((((j) - 12) & 1) + 4 * (((j) - 12) >> 1)) * 1024 + lane * 16); \
        MFV((j) == 0, av0, m, wv0[(j) & 1]); SB(); \
        if ((j) + 2 < 16) wv0[(j) & 1] = *(const LAS v4u*)(WV + ((j) + 2) * 1024); \
        MFV((j) == 0, av1, m, wv1[(j) & 1]); SB(); \
        if ((j) + 2 < 16) wv1[(j) & 1] = *(const LAS v4u*)(WV + 16384 + ((j) + 2) * 1024); \
      } while (0)
      QKV_STEP(0, mo[0]);
      QKV_STEP(1, mo[1]);
      for (unsigned spins = 0; __builtin_amdgcn_readfirstlane(min(min(fl.x, fl.y), min(fl.z, fl.w))) < (unsigned)s && spins < (1u << 22); ++spins) fl = *FL;
#pragma unroll
      for (int i = 0; i < 3; ++i) mf[i] = *(const volatile LAS v4u*)(MF + ((w + 1) & 3) * 2048 + i * 512);
      SB();
      QKV_STEP(2, mo[2]);
      QKV_STEP(3, mo[3]);
#pragma unroll
      for (int j = 4; j < 16; ++j) { const v4u m = mf[j & 3]; QKV_STEP(j, m); }
#undef QKV_STEP
    }
    asm("s_nop 7\n\ts_nop 1" : "+v"(aq0), "+v"(aq1), "+v"(ak0), "+v"(ak1), "+v"(av0), "+v"(av1));
    {
      const v4u kq = r < 8 ? pack8(ak0, ak1) : pack8(aq0, aq1);
      *(LAS v4u*)(L + CH_EX + (r >> 3) * 4096 + w * 512 + g * 128 + r8 * 16) = kq;
      *(LAS v4u*)(L + CH_EX + 2048 + w * 512 + (g & 1) * 256 + r * 16) = pack8(av0, av1);
    }
    __syncthreads();
    const LAS unsigned char* EXL = L + CH_EX + g * 128 + r8 * 16;
    f32x4 sx0, sx1, sm;
    {
      v4u qf[4], km[2];
#pragma unroll
      for (int i = 0; i < 4; ++i) qf[i] = *(const LAS v4u*)(EXL + 4096 + i * 512);
#pragma unroll
      for (int i = 0; i < 2; ++i) km[i] = *(const LAS v4u*)(EXL + i * 512);
#pragma unroll
      for (int i = 2; i < 4; ++i) kx[i] = *(const LAS v4u*)(L + CH_KV + i * 1024 + lane * 16);
      SB();
#pragma unroll
      for (int i = 0; i < 2; ++i) {
        MFV(i == 0, sx0, kx[i], qf[i]); MFV(i == 0, sx1, kx[4 + i], qf[i]);
        SB();
        kx[6 + i] = *(const LAS v4u*)(L + CH_KV + (6 + i) * 1024 + lane * 16);
        MFV(i == 0, sm, km[i], qf[i]);
        SB();
        km[i] = *(const LAS v4u*)(EXL + (2 + i) * 512);
      }
#pragma unroll
      for (int i = 2; i < 4; ++i) MFV(false, sx0, kx[i], qf[i]);
#pragma unroll
      for (int i = 2; i < 4; ++i) { MFV(false, sx1, kx[4 + i], qf[i]); MFV(false, sm, km[i - 2], qf[i]); }
      SB();
    }
    v4u vx[4], vma[4];
#pragma unroll
    for (int et = 0; et < 4; ++et) vx[et] = *(const LAS v4u*)(L + CH_KV + 8192 + et * 1024 + lane * 16);
#pragma unroll
    for (int m = 0; m < 4; ++m) vma[m] = *(const LAS v4u*)(L + CH_EX + 2048 + m * 512 + (g & 1) * 256 + r * 16);
    SB();
    mfma_settle3(sx0, sx1, sm);
    v4u p0, p1;
    {
      const bool lo = r < 8;
      const f32x4 sa = lo ? sx0 : sx1;
      unsigned u0 = pack2(sa.x, sa.y), u1 = pack2(sa.z, sa.w), u2 = pack2(lo ? sm.x : sm.z, lo ? sm.y : sm.w);
      u0 = pack2(bflo(u0) * kScale, bfhi(u0) * kScale);
      u1 = pack2(bflo(u1) * kScale, bfhi(u1) * kScale);
      u2 = pack2(bflo(u2) * kScale, bfhi(u2) * kScale);
      float a0 = bflo(u0), a1 = bfhi(u0), a2 = bflo(u1), a3 = bfhi(u1);
      float a4 = g < 2 ? bflo(u2) : kNegBig, a5 = g < 2 ? bfhi(u2) : kNegBig;
      float mx = fmaxf(fmaxf(fmaxf(a0, a1), fmaxf(a2, a3)), fmaxf(a4, a5));
      mx = fmaxf(mx, ror8f(mx));
      mx = xmax(mx);
      const float nm = -mx * kLog2e;
      a0 = __builtin_amdgcn_exp2f(fmaf(a0, kLog2e, nm)); a1 = __builtin_amdgcn_exp2f(fmaf(a1, kLog2e, nm));
      a2 = __builtin_amdgcn_exp2f(fmaf(a2, kLog2e, nm)); a3 = __builtin_amdgcn_exp2f(fmaf(a3, kLog2e, nm));
      a4 = __builtin_amdgcn_exp2f(fmaf(a4, kLog2e, nm)); a5 = __builtin_amdgcn_exp2f(fmaf(a5, kLog2e, nm));
      float sum = ((a0 + a1) + (a2 + a3)) + (a4 + a5);
      sum += ror8f(sum);
      sum = xsum(sum);
      const float inv = __builtin_amdgcn_rcpf(sum);
      u0 = pack2(a0 * inv, a1 * inv); u1 = pack2(a2 * inv, a3 * inv); u2 = pack2(a4 * inv, a5 * inv);
      const unsigned o0 = ror8(u0), o1 = ror8(u1), o2 = ror8(u2);
      p0.x = lo ? u0 : o0; p0.y = lo ? u1 : o1; p0.z = lo ? o0 : u0; p0.w = lo ? o1 : u1;
      p1.x = lo ? u2 : o2; p1.y = lo ? o2 : u2; p1.z = 0u; p1.w = 0u;
    }
    const v4u p1h = {0u, 0u, p1.x, p1.y};
    if (s + 1 < NSEG) {
      v4u of[4];
      f32x4 o2[8], pa[8];
      unsigned pk[8][2];
      const unsigned soff = p_region(p.ws, c) + (unsigned)((s & 1) * ROW);
      mfma_pv(o2[0], o2[1], vx[0], vx[1], vma[0], p0, p1, p1h);
      SB();
      vx[0] = *(const LAS v4u*)(L + CH_KV + 8192 + 4 * 1024 + lane * 16);
      vx[1] = *(const LAS v4u*)(L + CH_KV + 8192 + 5 * 1024 + lane * 16);
      mfma_pv(o2[2], o2[3], vx[2], vx[3], vma[1], p0, p1, p1h);
      SB();
      vx[2] = *(const LAS v4u*)(L + CH_KV + 8192 + 6 * 1024 + lane * 16);
      vx[3] = *(const LAS v4u*)(L + CH_KV + 8192 + 7 * 1024 + lane * 16);
      mfma_pv(o2[4], o2[5], vx[0], vx[1], vma[2], p0, p1, p1h);
      SB();
      of[0] = pack8(o2[0], o2[1]);
      SB();
      mfma_pv(o2[6], o2[7], vx[2], vx[3], vma[3], p0, p1, p1h);
      SB();
      of[1] = pack8(o2[2], o2[3]);
      SB();
#pragma unroll
      for (int t = 0; t < 8; ++t) {
        MFV(true, pa[t], wo[t][0], of[0]);
        SB();
        if (t == 4) { of[2] = pack8(o2[4], o2[5]); SB(); }
      }
#pragma unroll
      for (int t = 0; t < 8; ++t) {
        MFV(false, pa[t], wo[t][1], of[1]);
        SB();
        if (t == 2) { of[3] = pack8(o2[6], o2[7]); SB(); }
      }
#define PACK_TILE(t) do { pk[t][0] = min(pack2(pa[t].x, pa[t].y), 0xfffffffeu); pk[t][1] = min(pack2(pa[t].z, pa[t].w), 0xfffffffeu); \
                          asm("" : "+v"(pk[t][0]), "+v"(pk[t][1])); SB(); } while (0)
#pragma unroll
      for (int t = 0; t < 8; ++t) {
        MFV(false, pa[t], wo[t][2], of[2]);
        MFV(false, pa[t], wo[t][3], of[3]);
        SB();
        if (t >= 4) PACK_TILE(t - 4);
      }
      {
        const v4u o = {r < 8 ? pk[0][0] : pk[2][0], r < 8 ? pk[0][1] : pk[2][1], r < 8 ? pk[1][0] : pk[3][0], r < 8 ? pk[1][1] : pk[3][1]};
        st16s(rs, p_off[0], soff, o);
        SB();
      }
      PACK_TILE(4);
      PACK_TILE(5);
      asm("s_nop 1" : "+v"(pa[6]), "+v"(pa[7]));
      PACK_TILE(6);
      PACK_TILE(7);
#undef PACK_TILE
      const v4u o = {r < 8 ? pk[4][0] : pk[6][0], r < 8 ? pk[4][1] : pk[6][1], r < 8 ? pk[5][0] : pk[7][0], r < 8 ? pk[5][1] : pk[7][1]};
      st16s(rs, p_off[1], soff, o);
      SB();
    }
    st16(rs, kvm_base + (unsigned)(s * NH * 4096 + w * 1024 + lane * 16), *(const LAS v4u*)(L + CH_EX + w * 1024 + lane * 16));
    __syncthreads();
    const bool more = s + 1 < NSEG;
    const bool kv_known = more && have_qkv >= (unsigned)(s + 2);
    if (kv_known) {
      v4u v[4];
#pragma unroll
      for (int i = 0; i < 4; ++i) v[i] = ld16s(rs, (unsigned)(i * 1024 + lane * 16), qkv_base + (unsigned)((s + 1) * NH * 24 * 1024 + 4 * w * 1024));
      SB();
#pragma unroll
      for (int i = 0; i < 4; ++i) *(LAS v4u*)(L + CH_KV + (4 * w + i) * 1024 + lane * 16) = v[i];
    }
    DRAIN();
    __syncthreads();
    if (tid == 0) st4(rs, cnt_off(K_KM, c, h), (unsigned)(s + 1));
    if (!more) break;
    {
      if (!kv_known) {
        unsigned spins = 0;
        for (;;) {
          have_qkv = __builtin_amdgcn_readfirstlane(ld4(rs, cnt_off(K_QKV, c, h)));
          if (have_qkv >= (unsigned)(s + 2)) break;
          if (spin_fail(spins, rs)) break;
          __builtin_amdgcn_s_sleep(4);
        }
        __builtin_amdgcn_fence(__ATOMIC_ACQUIRE, "wavefront");
        v4u v[4];
#pragma unroll
        for (int i = 0; i < 4; ++i) v[i] = ld16s(rs, (unsigned)(i * 1024 + lane * 16), qkv_base + (unsigned)((s + 1) * NH * 24 * 1024 + 4 * w * 1024));
        SB();
#pragma unroll
        for (int i = 0; i < 4; ++i) *(LAS v4u*)(L + CH_KV + (4 * w + i) * 1024 + lane * 16) = v[i];
      } else if (have_qkv < (unsigned)(s + 3)) {
        have_qkv = __builtin_amdgcn_readfirstlane(ld4(rs, cnt_off(K_QKV, c, h)));
        __builtin_amdgcn_fence(__ATOMIC_ACQUIRE, "wavefront");
      }
#pragma unroll
      for (int j = 0; j < 2; ++j) {
        wv0[j] = *(const LAS v4u*)(L + CH_WV + 2 * w * 16384 + lane * 16 + j * 1024);
        wv1[j] = *(const LAS v4u*)(L + CH_WV + 2 * w * 16384 + 16384 + lane * 16 + j * 1024);
      }
      const unsigned slot = (unsigned)(((s + 1) % 3) * M_SLOT);
      unsigned spins = 0;
      for (;;) {
#pragma unroll
        for (int j = 0; j < 4; ++j) mo[j] = ld16s(rs, m_base + (unsigned)(j * 512), slot);
        SB();
        unsigned mx = 0;
#pragma unroll
        for (int j = 0; j < 4; ++j) mx = max(mx, max(max(mo[j].x, mo[j].y), max(mo[j].z, mo[j].w)));
        if (__all(mx != 0xffffffffu)) break;
        if (spin_fail(spins, rs)) break;
      }
      if (r < 8) {
#pragma unroll
        for (int j = 0; j < 4; ++j) *(LAS v4u*)(L + CH_MEM + (4 * w + j) * 512 + g * 128 + r * 16) = mo[j];
      }
      LDSWAIT();
      if (lane == 0) ((volatile LAS unsigned*)FL)[w] = (unsigned)(s + 1);
      asm volatile("" ::: "memory");
    }
  }
}

__device__ __forceinline__ void role_creduce(const Params& p, rsrc_t rs, LAS unsigned char* L, int c, int idx) {
  const int tid = threadIdx.x, lane = tid & 63, w = __builtin_amdgcn_readfirstlane(tid >> 6);
  const unsigned l16 = (unsigned)lane * 16u;
  const unsigned xcc = xcc_id();
  const unsigned blk = p_region(p.ws, c) + (unsigned)(idx * CR_BLK);
  unsigned col;
  {
    volatile LAS unsigned* T = (volatile LAS unsigned*)L;
    if (w == 0) {
      for (int i = 0; i < (c == 0 ? 0 : 4); ++i) __builtin_amdgcn_s_sleep(127);
      unsigned best = 0xffffffffu, arg = 0;
#pragma unroll 1
      for (int q = 0; q < 8 * (c == 0 ? CAL_N : 3); ++q) {
        const unsigned t0 = (unsigned)__builtin_amdgcn_s_memtime();
        unsigned y = ld4(rs, blk + (unsigned)((q & 7) * 4096 + (q >> 3) * 1024));
        asm volatile("s_waitcnt vmcnt(0)" : "+v"(y) :: "memory");
        const unsigned t1 = (unsigned)__builtin_amdgcn_s_memtime();
        if (t1 - t0 < best) { best = t1 - t0; arg = (unsigned)q & 7u; }
      }
      if (lane == 0) T[0] = arg;
    }
    __syncthreads();
    col = __builtin_amdgcn_readfirstlane(T[0]);
    const v4u o = {~0u, ~0u, ~0u, ~0u};
#pragma unroll
    for (int k = 0; k < 8; ++k) st16s(rs, l16 + (unsigned)(k & 3) * 1024u, blk + (unsigned)((2 * w + (k >> 2)) * ROW) + col * 4096u, o);
#pragma unroll
    for (int k = 0; k < 6; ++k) st16s(rs, l16 + (unsigned)w * 1024u, blk + (unsigned)(M_ROW0 + k * ROW) + col * 4096u, o);
    DRAIN();
    __syncthreads();
    if (tid == 0) st4(rs, cnt_off(K_COL, c, idx), 1u + col + 8u * xcc);
  }
  const unsigned p_base = blk + (unsigned)(2 * w * ROW) + col * 4096u;
  v4u sel;
  {
    const unsigned one = (lane & 1) ? 0x3f800000u : 0x00003f80u, q = ((unsigned)lane & 7u) >> 1;
    sel.x = q == 0u ? one : 0u; sel.y = q == 1u ? one : 0u; sel.z = q == 2u ? one : 0u; sel.w = q == 3u ? one : 0u;
  }
  unsigned m_off0, m_off1;
  {
    const unsigned tab = wait_table(rs, c, lane);
    const unsigned x0 = 2u * ((unsigned)lane >> 5) + (((unsigned)lane >> 3) & 1u);
    unsigned c0 = 0, c1 = 0;
#pragma unroll
    for (unsigned x = 0; x < 4; ++x) {
      const unsigned a = mem_copy(tab, x), b = mem_copy(tab, x + 4u);
      c0 = x0 == x ? a : c0; c1 = x0 == x ? b : c1;
    }
    const unsigned m_in = p_region(p.ws, c) + (unsigned)((idx >> 3) * ROW + (idx & 7) * 512 + w * 128 + (lane & 7) * 16 + ((lane >> 4) & 1) * 8);
    m_off0 = m_in + c0;
    m_off1 = m_in + c1;
  }
  for (int s = 0; s + 1 < NSEG; ++s) {
    const unsigned pb = p_base + (unsigned)((s & 1) * ROW);
    v4u x[4]; unsigned spins = 0;
    if (s > 0) __builtin_amdgcn_s_sleep(CR_SLEEP);
    for (;;) {
#pragma unroll
      for (int k = 0; k < 4; ++k) x[k] = ld16s(rs, l16 + (unsigned)k * 1024u, pb);
      SB();
      unsigned mx = 0;
#pragma unroll
      for (int k = 0; k < 4; ++k) mx = max(mx, max(max(x[k].x, x[k].y), max(x[k].z, x[k].w)));
      if (__all(mx != 0xffffffffu)) break;
      if (spin_fail(spins, rs)) break;
    }
    f32x4 a, a2;
    mfma_k2(a, sel, x[0], x[1]);
    mfma_k2(a2, sel, x[2], x[3]);
    mfma_settle2(a, a2);
    a += a2;
    a.x += ror8f(a.x); a.y += ror8f(a.y); a.z += ror8f(a.z); a.w += ror8f(a.w);
    v2u o; o.x = min(pack2(a.x, a.y), 0xfffffffeu); o.y = min(pack2(a.z, a.w), 0xfffffffeu);
    const unsigned slot = (unsigned)(((s + 1) % 3) * M_SLOT);
    st8(rs, m_off0 + slot, o);
    st8(rs, m_off1 + slot, o);
    __builtin_amdgcn_s_sleep(16);
    const v4u ones4 = {~0u, ~0u, ~0u, ~0u};
#pragma unroll
    for (int k = 0; k < 4; ++k) st16s(rs, l16 + (unsigned)k * 1024u, pb, ones4);
    const unsigned old = (unsigned)((s % 3) * M_SLOT);
    const v2u ones = {~0u, ~0u};
    st8(rs, m_off0 + old, ones);
    st8(rs, m_off1 + old, ones);
  }
}

__device__ __forceinline__ void role_qkv(const Params& p, rsrc_t rs, LAS unsigned char* L, int c, int h) {
  const int tid = threadIdx.x, lane = tid & 63, w = __builtin_amdgcn_readfirstlane(tid >> 6);
  const int g = lane >> 4, r = lane & 15;
  v4u wq[2][16], wk[2][16], wv[2][16];
#pragma unroll
  for (int t = 0; t < 2; ++t)
#pragma unroll
    for (int ks = 0; ks < 16; ++ks) {
      const unsigned o = (unsigned)(((h * 8 + 2 * w + t) * 16 + ks) * 1024 + lane * 16);
      wq[t][ks] = ld16p(rs, (unsigned)OFF_WQKV + o);
      wk[t][ks] = ld16p(rs, (unsigned)(OFF_WQKV + 4 * MB) + o);
      wv[t][ks] = ld16p(rs, (unsigned)(OFF_WQKV + 8 * MB) + o);
    }
  const int fb = r * RSTR + g * 16;
  volatile LAS unsigned* RDY = (volatile LAS unsigned*)(L + 2 * IMG);
  const unsigned xbase = (unsigned)(OFF_XA + (size_t)c * 2 * MB);
  bool staged = false;
  for (int s = 0; s < NSEG; ++s) {
    LAS unsigned char* X = L + (s & 1) * IMG;
    if (!staged) {
      if (c == 1) wait_cnt<POLL>(rs, cnt_off(K_X2, 0, 8 * w), 8, h & 7, (unsigned)(s + 1), lane);
      v4u v[8];
      rows_load(rs, xbase + (unsigned)s * 32768u, v, w, lane);
      rows_put(X, v, w, lane);
      __syncthreads();
    }
    unsigned nx = 0xffffffffu;
    if (c == 1 && w == 0) nx = ld4(rs, cnt_off(K_X2, 0, 0) + (unsigned)(lane & 31) * 128u);
    v4u nv[8];
    if (c == 0 && s + 1 < NSEG) rows_load(rs, xbase + (unsigned)(s + 1) * 32768u, nv, w, lane);
    f32x4 aq[2][2], ak[2][2], av[2][2];
    {
      v4u xf[4];
#pragma unroll
      for (int i = 0; i < 4; ++i) xf[i] = row_frag(X, i & 1, i >> 1, fb);
      SB();
#pragma unroll
      for (int i = 0; i < 32; ++i) {
        const int ks = i >> 1, tt = i & 1;
        const v4u x = xf[i & 3];
        MFA(ks == 0, aq[0][tt], wq[0][ks], x);
        MFA(ks == 0, aq[1][tt], wq[1][ks], x);
        MFA(ks == 0, ak[0][tt], wk[0][ks], x);
        SB();
        if (i >= 1 && i + 3 < 32) xf[(i + 3) & 3] = row_frag(X, (i + 3) & 1, (i + 3) >> 1, fb);
        MFA(ks == 0, ak[1][tt], wk[1][ks], x);
        MFV(ks == 0, av[0][tt], x, wv[0][ks]);
        MFV(ks == 0, av[1][tt], x, wv[1][ks]);
        SB();
      }
    }
    mfma_settle(av[0][0], av[1][0], av[0][1], av[1][1]);
    mfma_settle(aq[0][0], aq[1][0], ak[0][0], ak[1][0]);
    mfma_settle(aq[0][1], aq[1][1], ak[0][1], ak[1][1]);
    const unsigned ob = (unsigned)(OFF_QKVF + (size_t)((c * NSEG + s) * NH + h) * 24 * KB) + (unsigned)lane * 16u;
#pragma unroll
    for (int tt = 0; tt < 2; ++tt) {
      st16(rs, ob + (unsigned)(8192 + (tt * 4 + w) * 1024), pack8(ak[0][tt], ak[1][tt]));
      st16(rs, ob + (unsigned)((tt * 4 + w) * 1024), pack8(aq[0][tt], aq[1][tt]));
    }
#pragma unroll
    for (int t = 0; t < 2; ++t) st16(rs, ob + (unsigned)(16384 + (2 * w + t) * 1024), pack8(av[t][0], av[t][1]));
    if (c == 0) {
      if (s + 1 < NSEG) rows_put(L + ((s + 1) & 1) * IMG, nv, w, lane);
      staged = true;
    } else if (w == 0) {
      const bool ok = __all(nx >= (unsigned)(s + 2)) && s + 1 < NSEG;
      if (lane == 0) RDY[0] = ok ? 1u : 0u;
    }
    DRAIN();
    __syncthreads();
    if (tid == 0) st4(rs, cnt_off(K_QKV, c, h), (unsigned)(s + 1));
    if (c == 1) {
      staged = RDY[0] != 0u;
      if (staged) {
        v4u v[8];
        rows_load(rs, xbase + (unsigned)(s + 1) * 32768u, v, w, lane);
        rows_put(L + ((s + 1) & 1) * IMG, v, w, lane);
        __syncthreads();
      }
    }
  }
}

__device__ __forceinline__ void role_att(const Params& p, rsrc_t rs, LAS unsigned char* L, int c, int pp) {
  const int tid = threadIdx.x, lane = tid & 63, w = __builtin_amdgcn_readfirstlane(tid >> 6);
  const int g = lane >> 4, r = lane & 15, r8 = lane & 7;
  v4u wo[2][8][4];
#pragma unroll
  for (int hh = 0; hh < 2; ++hh)
#pragma unroll
    for (int dt = 0; dt < 8; ++dt)
#pragma unroll
      for (int ks = 0; ks < 4; ++ks)
        wo[hh][dt][ks] = ld16p(rs, (unsigned)OFF_WO + (unsigned)((((2 * pp + hh) * 32 + 8 * w + dt) * 4 + ks) * 1024 + lane * 16));
  const int hh = w >> 1, tt = w & 1, h = 2 * pp + hh;
  const int q = 16 * tt + r;
  unsigned ok0 = 0, ok1 = 0;
#pragma unroll
  for (int j = 0; j < 4; ++j) { ok0 |= (4 * g + j <= q ? 1u : 0u) << j; ok1 |= (16 + 4 * g + j <= q ? 1u : 0u) << j; }
  LAS unsigned char* EX = L;
  LAS unsigned char* TR = L + 16384;
  const unsigned qbase = (unsigned)(OFF_QKVF + (size_t)(c * NSEG * NH + h) * 24 * KB);
  const unsigned mbase = (unsigned)(OFF_KVM + (size_t)(c * NSEG * NH + h) * 4 * KB);
  const unsigned l16 = (unsigned)lane * 16u;
  unsigned have_qkv = 0, have_km = 0;
  v4u qf[4], kf[8], vf[8];
  bool loaded = false;
  for (int s = 0; s < NSEG; ++s) {
    if (!loaded) {
      if (have_qkv < (unsigned)(s + 1)) wait_cnt<8>(rs, cnt_off(K_QKV, c, h), 1, 0, (unsigned)(s + 1), lane);
      const unsigned ib = qbase + (unsigned)(s * NH * 24 * 1024);
#pragma unroll
      for (int ks = 0; ks < 4; ++ks) qf[ks] = ld16s(rs, l16 + (unsigned)(ks * 1024), ib + (unsigned)(tt * 4096));
#pragma unroll
      for (int i = 0; i < 8; ++i) kf[i] = ld16s(rs, l16 + (unsigned)((i & 3) * 1024), ib + (unsigned)(8192 + (i & 4) * 1024));
#pragma unroll
      for (int i = 0; i < 8; ++i) vf[i] = ld16s(rs, l16 + (unsigned)((i & 3) * 1024), ib + (unsigned)(16384 + (i & 4) * 1024));
      SB();
    }
    f32x4 sx0, sx1, sm;
#pragma unroll
    for (int ks = 0; ks < 4; ++ks) { MFV(ks == 0, sx0, kf[ks], qf[ks]); MFV(ks == 0, sx1, kf[4 + ks], qf[ks]); }
    SB();
    if (have_km < (unsigned)(s + 1)) wait_cnt<POLL>(rs, cnt_off(K_KM, c, h), 1, 0, (unsigned)(s + 1), lane);
    const unsigned mb = mbase + (unsigned)(s * NH * 4096);
    v4u km[4], vma[4];
#pragma unroll
    for (int ks = 0; ks < 4; ++ks) km[ks] = ld16s(rs, (unsigned)(ks * 512 + g * 128 + r8 * 16), mb);
#pragma unroll
    for (int m = 0; m < 4; ++m) vma[m] = ld16s(rs, (unsigned)(2048 + m * 512 + (g & 1) * 256 + r * 16), mb);
    const unsigned nq = ld4(rs, cnt_off(K_QKV, c, h));
    SB();
#pragma unroll
    for (int ks = 0; ks < 4; ++ks) MFV(ks == 0, sm, km[ks], qf[ks]);
    mfma_settle3(sx0, sx1, sm);
    v4u p0, p1;
    softmax_frags(sx0, sx1, sm, ok0, ok1, g < 2, p0, p1);
    const v4u p1h = {0u, 0u, p1.x, p1.y};
    {
      f32x4 o2[8];
#pragma unroll
      for (int m = 0; m < 4; ++m)
        mfma_pv(o2[2 * m], o2[2 * m + 1], vf[2 * m], vf[2 * m + 1], vma[m], p0, p1, p1h);
      mfma_settle(o2[0], o2[1], o2[2], o2[3]);
      mfma_settle(o2[4], o2[5], o2[6], o2[7]);
#pragma unroll
      for (int m = 0; m < 4; ++m) *(LAS v4u*)(EX + (w * 4 + m) * 1024 + lane * 16) = pack8(o2[2 * m], o2[2 * m + 1]);
    }
    have_qkv = __builtin_amdgcn_readfirstlane(nq);
    loaded = s + 1 < NSEG && have_qkv >= (unsigned)(s + 2);
    __syncthreads();
    f32x4 acc[8][2];
    const unsigned pout = (unsigned)(OFF_PO + (size_t)(s * NATT + pp) * 32 * KB);
    {
      const unsigned ib = qbase + (unsigned)((loaded ? s + 1 : s) * NH * 24 * 1024);
      v4u of[4], rw;
#define OFRAG(i) ((((i) >> 2) & 1) * 8 + ((i) >> 3) * 4 + ((i) & 3))
#pragma unroll
      for (int i = 0; i < 4; ++i) of[i] = *(const LAS v4u*)(EX + OFRAG(i) * 1024 + lane * 16);
      SB();
#pragma unroll
      for (int i = 0; i < 16; ++i) {
        const int t2 = i >> 3, h2 = (i >> 2) & 1, ks = i & 3;
        const bool first = h2 == 0 && ks == 0;
        const v4u x = of[i & 3];
#pragma unroll
        for (int dt = 0; dt < 4; ++dt) MFA(first, acc[dt][t2], wo[h2][dt][ks], x);
        SB();
        if (i >= 1 && i + 3 < 16) of[(i + 3) & 3] = *(const LAS v4u*)(EX + OFRAG(i + 3) * 1024 + lane * 16);
        if (i == 9) partial_put(TR, acc, 0, w, lane);
        if (i >= 11 && i < 15) rw = partial_row(TR, 0, i - 11, w, lane);
        MFA(first, acc[4][t2], wo[h2][4][ks], x);
        MFA(first, acc[5][t2], wo[h2][5][ks], x);
        SB();
        if (i < 4) qf[i] = ld16s(rs, l16 + (unsigned)(i * 1024), ib + (unsigned)(tt * 4096));
        else if (i < 12) kf[i - 4] = ld16s(rs, l16 + (unsigned)(((i - 4) & 3) * 1024), ib + (unsigned)(8192 + ((i - 4) & 4) * 1024));
        else vf[i - 12] = ld16s(rs, l16 + (unsigned)((i - 12) * 1024), ib + 16384u);
        MFA(first, acc[6][t2], wo[h2][6][ks], x);
        SB();
        if (i >= 12) vf[i - 8] = ld16s(rs, l16 + (unsigned)((i - 12) * 1024), ib + 20480u);
        if (i >= 11 && i < 15) partial_row_store(rs, pout, rw, 0, i - 11, w, lane);
        MFA(first, acc[7][t2], wo[h2][7][ks], x);
        SB();
        if (i == 10) __syncthreads();
      }
#undef OFRAG
    }
    mfma_settle(acc[0][1], acc[1][1], acc[2][1], acc[3][1]);
    mfma_settle(acc[4][1], acc[5][1], acc[6][1], acc[7][1]);
    partial_put(TR, acc, 1, w, lane);
    __syncthreads();
    partial_store(rs, pout, TR, 1, w, lane);
    const unsigned nk = ld4(rs, cnt_off(K_KM, c, h));
    DRAIN();
    have_km = __builtin_amdgcn_readfirstlane(nk);
    __syncthreads();
    if (tid == 0) st4(rs, cnt_off(K_ATT, c, pp), (unsigned)(s + 1));
  }
}

__device__ __forceinline__ float gelu_tanh(float x) {
  const float z = x * fmaf(x * x, -2.0f * 0.7978845608028654f * 0.044715f * 1.4426950408889634f, -2.0f * 0.7978845608028654f * 1.4426950408889634f);
  return x * __builtin_amdgcn_rcpf(1.0f + __builtin_amdgcn_exp2f(z));
}
__device__ __forceinline__ unsigned geglu2(float g0, float g1, float u0, float u1) {
  const unsigned gp = pack2(g0, g1), up = pack2(u0, u1);
  const unsigned ap = pack2(gelu_tanh(bflo(gp)), gelu_tanh(bfhi(gp)));
  return pack2(bflo(ap) * bflo(up), bfhi(ap) * bfhi(up));
}
__device__ __forceinline__ void role_ffn(const Params& p, rsrc_t rs, LAS unsigned char* L, int c, int sl) {
  const int tid = threadIdx.x, lane = tid & 63, w = __builtin_amdgcn_readfirstlane(tid >> 6);
  const int g = lane >> 4, r = lane & 15;
  v4u wg[2][16], wu[2][16], wd[8][4];
  stream_halves<4>(L, DFF, w, lane,
      [&](int m) { return (m == 0 ? p.wg : p.wu) + sl * 128; },
      [&](int i, LAS unsigned char* img) {
#pragma unroll
        for (int ks = 0; ks < 16; ++ks) {
          const v4u f = half_frag(img, w, ks, lane);
          if (i < 2) wg[i & 1][ks] = f; else wu[i & 1][ks] = f;
        }
      });
  wave_panel(p.wd + (size_t)sl * 128 * DM + w * 128, DM, L + w * PSTG, lane, [&](int t, int ks, v4u f) { wd[t][ks] = f; });
  __syncthreads();
  LAS unsigned char* X = L;
  LAS unsigned char* EX = L + IMG;
  LAS unsigned char* TR = L + IMG + 8192;
  const int fb = r * RSTR + g * 16;
  volatile LAS unsigned* RDY = (volatile LAS unsigned*)(L + 2 * IMG + 8192);
  const unsigned xbase = (unsigned)(OFF_XF + (size_t)c * 2 * MB);
  bool staged = false;
  for (int s = 0; s < NSEG; ++s) {
    if (!staged) {
      wait_cnt<POLL>(rs, cnt_off(K_X1, c, 8 * w), 8, sl & 7, (unsigned)(s + 1), lane);
      v4u v[8];
      rows_load(rs, xbase + (unsigned)s * 32768u, v, w, lane);
      rows_put(X, v, w, lane);
      __syncthreads();
    }
    f32x4 ag[2][2], au[2][2];
    {
      v4u xf[4];
#pragma unroll
      for (int i = 0; i < 4; ++i) xf[i] = row_frag(X, 0, i, fb);
      SB();
      unsigned hv[4];
#pragma unroll
      for (int i = 0; i < 32; ++i) {
        const int ks = i & 15, tt = i >> 4;
        const v4u x = xf[i & 3];
        MFA(ks == 0, ag[0][tt], wg[0][ks], x);
        MFA(ks == 0, ag[1][tt], wg[1][ks], x);
        SB();
        if (i >= 1 && i + 3 < 32) xf[(i + 3) & 3] = row_frag(X, (i + 3) >> 4, (i + 3) & 15, fb);
        MFA(ks == 0, au[0][tt], wu[0][ks], x);
        MFA(ks == 0, au[1][tt], wu[1][ks], x);
        SB();
        if (i >= 19 && i < 27 && (i & 1)) {
          const int q = (i - 19) >> 1, t = q >> 1, j = (q & 1) * 2;
          hv[q] = geglu2(ag[t][0][j], ag[t][0][j + 1], au[t][0][j], au[t][0][j + 1]);
          SB();
        }
        if (i == 27) { *(LAS v4u*)(EX + w * 1024 + lane * 16) = v4u{hv[0], hv[1], hv[2], hv[3]}; SB(); }
      }
    }
    __syncthreads();
    const unsigned pout = (unsigned)(OFF_PF + (size_t)(s * NFFN + sl) * 32 * KB);
    f32x4 acc[8][2];
    unsigned nx = 0;
    {
      v4u hf[4];
#pragma unroll
      for (int i = 0; i < 4; ++i) hf[i] = *(const LAS v4u*)(EX + i * 1024 + lane * 16);
      mfma_settle(ag[0][1], ag[1][1], au[0][1], au[1][1]);
      SB();
      unsigned hv[4];
#pragma unroll
      for (int ks = 0; ks < 4; ++ks) {
        const int t = ks >> 1, j = (ks & 1) * 2;
        const unsigned gp = pack2(ag[t][1][j], ag[t][1][j + 1]), up = pack2(au[t][1][j], au[t][1][j + 1]);
        MFV(ks == 0, acc[0][0], wd[0][ks], hf[ks]); MFV(ks == 0, acc[1][0], wd[1][ks], hf[ks]);
        SB();
        const float g0 = gelu_tanh(bflo(gp));
        MFV(ks == 0, acc[2][0], wd[2][ks], hf[ks]); MFV(ks == 0, acc[3][0], wd[3][ks], hf[ks]);
        SB();
        const float g1 = gelu_tanh(bfhi(gp));
        MFV(ks == 0, acc[4][0], wd[4][ks], hf[ks]); MFV(ks == 0, acc[5][0], wd[5][ks], hf[ks]);
        SB();
        const unsigned ap = pack2(g0, g1);
        hv[ks] = pack2(bflo(ap) * bflo(up), bfhi(ap) * bfhi(up));
        MFV(ks == 0, acc[6][0], wd[6][ks], hf[ks]); MFV(ks == 0, acc[7][0], wd[7][ks], hf[ks]);
        SB();
      }
      *(LAS v4u*)(EX + (4 + w) * 1024 + lane * 16) = v4u{hv[0], hv[1], hv[2], hv[3]};
      mfma_settle(acc[0][0], acc[1][0], acc[2][0], acc[3][0]);
      mfma_settle(acc[4][0], acc[5][0], acc[6][0], acc[7][0]);
      partial_put(TR, acc, 0, w, lane);
      if (w == 0) nx = ld4(rs, cnt_off(K_X1, c, 0) + (unsigned)(lane & 31) * 128u);
      __syncthreads();
#pragma unroll
      for (int i = 0; i < 4; ++i) hf[i] = *(const LAS v4u*)(EX + (4 + i) * 1024 + lane * 16);
      v4u rw = partial_row(TR, 0, 0, w, lane);
      SB();
#pragma unroll
      for (int ks = 0; ks < 4; ++ks) {
#pragma unroll
        for (int dt = 0; dt < 4; ++dt) MFV(ks == 0, acc[dt][1], wd[dt][ks], hf[ks]);
        SB();
        partial_row_store(rs, pout, rw, 0, ks, w, lane);
        MFV(ks == 0, acc[4][1], wd[4][ks], hf[ks]); MFV(ks == 0, acc[5][1], wd[5][ks], hf[ks]);
        SB();
        if (ks < 3) rw = partial_row(TR, 0, ks + 1, w, lane);
        MFV(ks == 0, acc[6][1], wd[6][ks], hf[ks]); MFV(ks == 0, acc[7][1], wd[7][ks], hf[ks]);
        SB();
      }
    }
    mfma_settle(acc[0][1], acc[1][1], acc[2][1], acc[3][1]);
    mfma_settle(acc[4][1], acc[5][1], acc[6][1], acc[7][1]);
    partial_put(TR, acc, 1, w, lane);
    if (w == 0) {
      const bool ok = __all(nx >= (unsigned)(s + 2)) && s + 1 < NSEG;
      if (lane == 0) RDY[0] = ok ? 1u : 0u;
    }
    __syncthreads();
    staged = RDY[0] != 0u;
    if (staged) {
      v4u nv[8];
      partial_store(rs, pout, TR, 1, w, lane);
      rows_load(rs, xbase + (unsigned)(s + 1) * 32768u, nv, w, lane);
      rows_put(X, nv, w, lane);
      DRAIN();
      __syncthreads();
      if (tid == 0) st4(rs, cnt_off(K_FFN, c, sl), (unsigned)(s + 1));
    } else {
      partial_store(rs, pout, TR, 1, w, lane);
      DRAIN();
      __syncthreads();
      if (tid == 0) st4(rs, cnt_off(K_FFN, c, sl), (unsigned)(s + 1));
    }
  }
}

template <int NP>
__device__ __forceinline__ void sum_rows(rsrc_t rs, unsigned off, float (&a)[8]) {
  v4u v[NP];
#pragma unroll
  for (int i = 0; i < NP; ++i) v[i] = ld16(rs, off + (unsigned)i * 32768u);
  SB();
#pragma unroll
  for (int j = 0; j < 8; ++j) a[j] = 0.f;
  unsigned c0 = 0x00003f80u, c1 = 0x3f800000u;
  asm("" : "+v"(c0), "+v"(c1));
  const bf16x2 e0 = __builtin_bit_cast(bf16x2, c0), e1 = __builtin_bit_cast(bf16x2, c1);
#pragma unroll
  for (int i = 0; i < NP; ++i) {
#pragma unroll
    for (int q = 0; q < 4; ++q) {
      const unsigned xu = v[i][q];
      const bf16x2 x = __builtin_bit_cast(bf16x2, xu);
      a[2 * q] = __builtin_amdgcn_fdot2_f32_bf16(x, e0, a[2 * q], false);
      a[2 * q + 1] = __builtin_amdgcn_fdot2_f32_bf16(x, e1, a[2 * q + 1], false);
    }
  }
}
__device__ __forceinline__ void role_red(const Params& p, rsrc_t rs, LAS unsigned char* L, int t) {
  const int tid = threadIdx.x, lane = tid & 63, w = __builtin_amdgcn_readfirstlane(tid >> 6);
  const int c = w >> 1, ffn = w & 1;
  for (int s = 0; s < NSEG; ++s) {
    float a[8];
    if (!ffn) {
      wait_cnt<POLL>(rs, cnt_off(K_ATT, c, 0), NATT, t & 15, (unsigned)(s + 1), lane);
      sum_rows<NATT>(rs, (unsigned)(OFF_PO + (size_t)s * NATT * 32 * KB) + (unsigned)(t * 1024 + lane * 16), a);
    } else {
      wait_cnt<POLL>(rs, cnt_off(K_FFN, c, 0), NFFN, t % NFFN, (unsigned)(s + 1), lane);
      sum_rows<NFFN>(rs, (unsigned)(OFF_PF + (size_t)s * NFFN * 32 * KB) + (unsigned)(t * 1024 + lane * 16), a);
    }
    v4u xo;
    if (w == 0) xo = *(const v4u*)(p.x + (size_t)(s * SEG + t) * DM + lane * 8);
    else xo = *(volatile LAS v4u*)(L + s * 1024 + lane * 16);
    float x[8] = {bflo(xo.x), bfhi(xo.x), bflo(xo.y), bfhi(xo.y), bflo(xo.z), bfhi(xo.z), bflo(xo.w), bfhi(xo.w)};
#pragma unroll
    for (int j = 0; j < 8; ++j) x[j] = bf16r(x[j] + bf16r(a[j]));
    v4u xn; xn.x = pack2(x[0], x[1]); xn.y = pack2(x[2], x[3]); xn.z = pack2(x[4], x[5]); xn.w = pack2(x[6], x[7]);
    if (w == 3) {
      *(v4u*)(p.out + (size_t)(s * SEG + t) * DM + lane * 8) = xn;
    } else {
      *(volatile LAS v4u*)(L + s * 1024 + lane * 16) = xn;
      float y[8];
      layernorm8(x, y);
      v4u o; o.x = pack2(y[0], y[1]); o.y = pack2(y[2], y[3]); o.z = pack2(y[4], y[5]); o.w = pack2(y[6], y[7]);
      const unsigned dst = (ffn ? (unsigned)(OFF_XA + 2 * MB) : (unsigned)(OFF_XF + (size_t)c * 2 * MB)) + (unsigned)((s * SEG + t) * 1024 + lane * 16);
      st16(rs, dst, o);
      LDSWAIT();
      DRAIN();
      const unsigned f = ffn ? cnt_off(K_X2, 0, t) : cnt_off(K_X1, c, t);
      if (lane == 0) st4(rs, f, (unsigned)(s + 1));
    }
  }
}

__global__ void __launch_bounds__(256, 1) k_main(Params p) {
  __shared__ __attribute__((aligned(16))) unsigned char lds[LDS_BYTES];
  LAS unsigned char* L = (LAS unsigned char*)lds;
  const rsrc_t rs = __builtin_amdgcn_make_buffer_rsrc((void*)p.ws, 0, (int)WS_BYTES, 0x00020000);
  const int b = blockIdx.x;
  if (b >= 192 && b < 220)
    for (int i = 0; i < LATE; ++i) __builtin_amdgcn_s_sleep(100);
  if (b < 64) role_chain(p, rs, L, b >> 5, b & 31);
  else if (b < 96) role_creduce(p, rs, L, (b - 64) >> 4, (b - 64) & 15);
  else if (b < 160) role_qkv(p, rs, L, (b - 96) >> 5, (b - 96) & 31);
  else if (b < 192) role_att(p, rs, L, (b - 160) >> 4, (b - 160) & 15);
  else if (b < 220) role_ffn(p, rs, L, (b - 192) / NFFN, (b - 192) % NFFN);
  else if (b < 252) role_red(p, rs, L, b - 220);
}

extern "C" void kernel_launch(void* const* d_in, const int* in_sizes, int n_in,
                              void* d_out, int out_size, void* d_ws, size_t ws_size,
                              hipStream_t stream) {
  PrepParams pp;
  pp.x = (const u16*)d_in[0];
  pp.wq = (const u16*)d_in[2];
  pp.wk = (const u16*)d_in[3];
  pp.wv = (const u16*)d_in[4];
  pp.wo = (const u16*)d_in[5];
  pp.ws = (unsigned char*)d_ws;
  k_prep<<<NJ_ALL, 256, 0, stream>>>(pp);
  Params p;
  p.x = (const u16*)d_in[0];
  p.mem0 = (const u16*)d_in[1];
  p.wg = (const u16*)d_in[8];
  p.wu = (const u16*)d_in[9];
  p.wd = (const u16*)d_in[10];
  p.out = (u16*)d_out;
  p.ws = (unsigned char*)d_ws;
  k_main<<<252, 256, 0, stream>>>(p);
}
```

```cpp
#include <hip/hip_runtime.h>
#include <hip/hip_bf16.h>
#include <stdint.h>

#define GAS __attribute__((address_space(1)))
#define LAS __attribute__((address_space(3)))
typedef unsigned v4u __attribute__((ext_vector_type(4)));
typedef unsigned v2u __attribute__((ext_vector_type(2)));
typedef float f32x4 __attribute__((ext_vector_type(4)));
typedef float f32x2 __attribute__((ext_vector_type(2)));
typedef short bf16x8 __attribute__((ext_vector_type(8)));
typedef __bf16 bf16x2 __attribute__((ext_vector_type(2)));
typedef GAS unsigned gu32;
typedef unsigned short u16;
#define RLX_AGENT __ATOMIC_RELAXED, __HIP_MEMORY_SCOPE_AGENT
#define SB() __builtin_amdgcn_sched_barrier(0)
#define DRAIN() asm volatile("s_waitcnt vmcnt(0)" ::: "memory")
#define LDSWAIT() asm volatile("s_waitcnt lgkmcnt(0)" ::: "memory")

constexpr int S_TOK = 2048, DM = 512, NH = 32, DH = 128, NMEM = 8, SEG = 32, NSEG = 64, DFF = 1792;
constexpr int NFFN = 14, NATT = 16;

constexpr size_t KB = 1024, MB = 1024 * 1024;
constexpr size_t OFF_ABORT = 0;
enum { K_QKV = 0, K_KM = 1, K_ATT = 2, K_X1 = 3, K_FFN = 4, K_X2 = 5, K_COL = 6 };
constexpr size_t OFF_CNT = 4 * KB;
__host__ __device__ constexpr unsigned cnt_off(int kind, int c, int idx) { return (unsigned)(OFF_CNT + (size_t)((kind * 2 + c) * 32 + idx) * 128); }
constexpr size_t ZERO_BYTES = 64 * KB;
constexpr size_t ROW = 32 * KB;
constexpr size_t OFF_WQKV = 2 * MB;
constexpr size_t OFF_WO = OFF_WQKV + 12 * MB;
constexpr size_t OFF_XA = 24 * MB;
constexpr size_t OFF_XF = OFF_XA + 4 * MB;
constexpr size_t OFF_KVM = OFF_XF + 4 * MB;
constexpr size_t OFF_QKVF = OFF_KVM + 16 * MB;
constexpr size_t OFF_PO = OFF_QKVF + 96 * MB;
constexpr size_t OFF_PF = OFF_PO + 32 * MB;
constexpr size_t OFF_P = OFF_PF + 28 * MB;
constexpr size_t CR_BLK = 16 * ROW, P_CHAIN = 16 * CR_BLK, M_ROW0 = 8 * ROW, M_SLOT = 2 * ROW;
constexpr size_t WS_BYTES = OFF_P + 2 * P_CHAIN + CR_BLK;
__device__ __forceinline__ unsigned p_region(const unsigned char* ws, int c) {
  return (unsigned)OFF_P + (unsigned)((0u - (unsigned)(uintptr_t)ws - (unsigned)OFF_P) & (unsigned)(CR_BLK - 1)) + (unsigned)c * (unsigned)P_CHAIN;
}
static_assert(OFF_CNT + 14 * 32 * 128 <= ZERO_BYTES, "flags");
static_assert(OFF_WO + 4 * MB <= OFF_XA, "weights");
static_assert(WS_BYTES <= 256 * MB, "workspace");

constexpr unsigned kSpinCap = 1u << 17;
constexpr int POLL = 3;
constexpr int CAL_N = 2;
constexpr int LATE = 2;
constexpr int CR_SLEEP = 56;
constexpr int LDS_BYTES = 163840;

struct Params {
  const u16* x;
  const u16* mem0;
  const u16* wg;
  const u16* wu;
  const u16* wd;
  u16* out;
  unsigned char* ws;
};
struct PrepParams {
  const u16* x;
  const u16* wq;
  const u16* wk;
  const u16* wv;
  const u16* wo;
  unsigned char* ws;
};

__device__ __forceinline__ unsigned xcc_id() { return (unsigned)__builtin_amdgcn_s_getreg(20 | (3 << 11)) & 7u; }
__device__ __forceinline__ unsigned pack2(float a, float b) {
  f32x2 v = {a, b};
  bf16x2 r = __builtin_convertvector(v, bf16x2);
  return __builtin_bit_cast(unsigned, r);
}
__device__ __forceinline__ float bf16r(float a) { return __uint_as_float(pack2(a, 0.f) << 16); }
__device__ __forceinline__ float bflo(unsigned u) { return __uint_as_float(u << 16); }
__device__ __forceinline__ float bfhi(unsigned u) { return __uint_as_float(u & 0xffff0000u); }
#define MFMA "v_mfma_f32_16x16x32_bf16 "
template <bool FIRST> __device__ __forceinline__ void mfa(f32x4& c, v4u a, v4u b) {
  if (FIRST) asm(MFMA "%0, %1, %2, 0" : "=&v"(c) : "a"(a), "v"(b));
  else asm(MFMA "%0, %1, %2, %0" : "+v"(c) : "a"(a), "v"(b));
}
template <bool FIRST> __device__ __forceinline__ void mfv(f32x4& c, v4u a, v4u b) {
  if (FIRST) asm(MFMA "%0, %1, %2, 0" : "=&v"(c) : "v"(a), "v"(b));
  else asm(MFMA "%0, %1, %2, %0" : "+v"(c) : "v"(a), "v"(b));
}
#define MFA(first, c, a, b) do { if (first) mfa<true>(c, a, b); else mfa<false>(c, a, b); } while (0)
#define MFV(first, c, a, b) do { if (first) mfv<true>(c, a, b); else mfv<false>(c, a, b); } while (0)
__device__ __forceinline__ void mfma_pv(f32x4& c0, f32x4& c1, v4u a0, v4u a1, v4u m, v4u p0, v4u pl, v4u ph) {
  asm("s_nop 1\n\t"
      MFMA "%0, %2, %5, 0\n\t"
      MFMA "%1, %3, %5, 0\n\t"
      MFMA "%0, %4, %6, %0\n\t"
      MFMA "%1, %4, %7, %1"
      : "=&v"(c0), "=&v"(c1)
      : "v"(a0), "v"(a1), "v"(m), "v"(p0), "v"(pl), "v"(ph));
}
__device__ __forceinline__ void mfma_k2(f32x4& c, v4u a, v4u b0, v4u b1) {
  asm("s_nop 1\n\t"
      MFMA "%0, %1, %2, 0\n\t"
      MFMA "%0, %1, %3, %0"
      : "=&v"(c)
      : "v"(a), "v"(b0), "v"(b1));
}
__device__ __forceinline__ void mfma_settle2(f32x4& c0, f32x4& c1) { asm("s_nop 7\n\ts_nop 1" : "+v"(c0), "+v"(c1)); }
__device__ __forceinline__ void mfma_settle3(f32x4& c0, f32x4& c1, f32x4& c2) { asm("s_nop 7\n\ts_nop 1" : "+v"(c0), "+v"(c1), "+v"(c2)); }
__device__ __forceinline__ void mfma_settle(f32x4& c0, f32x4& c1, f32x4& c2, f32x4& c3) {
  asm("s_nop 7\n\ts_nop 1" : "+v"(c0), "+v"(c1), "+v"(c2), "+v"(c3));
}
__device__ __forceinline__ v4u pack8(f32x4 a, f32x4 b) {
  v4u r;
  r.x = pack2(a.x, a.y); r.y = pack2(a.z, a.w); r.z = pack2(b.x, b.y); r.w = pack2(b.z, b.w);
  return r;
}
__device__ __forceinline__ float xmax(float x) {
  auto s = __builtin_amdgcn_permlane16_swap(__float_as_uint(x), __float_as_uint(x), false, false);
  x = fmaxf(__uint_as_float(s[0]), __uint_as_float(s[1]));
  auto t = __builtin_amdgcn_permlane32_swap(__float_as_uint(x), __float_as_uint(x), false, false);
  return fmaxf(__uint_as_float(t[0]), __uint_as_float(t[1]));
}
__device__ __forceinline__ float xsum(float x) {
  auto s = __builtin_amdgcn_permlane16_swap(__float_as_uint(x), __float_as_uint(x), false, false);
  x = __uint_as_float(s[0]) + __uint_as_float(s[1]);
  auto t = __builtin_amdgcn_permlane32_swap(__float_as_uint(x), __float_as_uint(x), false, false);
  return __uint_as_float(t[0]) + __uint_as_float(t[1]);
}
__device__ __forceinline__ unsigned ror8(unsigned x) {
  return (unsigned)__builtin_amdgcn_update_dpp(0, (int)x, 0x128, 0xf, 0xf, false);
}
__device__ __forceinline__ float ror8f(float x) { return __uint_as_float(ror8(__float_as_uint(x))); }
template <int CTRL> __device__ __forceinline__ float dppf(float x) {
  return __uint_as_float((unsigned)__builtin_amdgcn_update_dpp(0, (int)__float_as_uint(x), CTRL, 0xf, 0xf, false));
}
__device__ __forceinline__ float wave_sum(float x) {
  x += dppf<0x121>(x); x += dppf<0x122>(x); x += dppf<0x124>(x); x += dppf<0x128>(x);
  return xsum(x);
}
typedef __amdgpu_buffer_rsrc_t rsrc_t;
__device__ __forceinline__ v4u ld16(rsrc_t rs, unsigned off) { return __builtin_amdgcn_raw_buffer_load_b128(rs, (int)off, 0, 16); }
__device__ __forceinline__ v4u ld16s(rsrc_t rs, unsigned voff, unsigned soff) { return __builtin_amdgcn_raw_buffer_load_b128(rs, (int)voff, (int)soff, 16); }
__device__ __forceinline__ v2u ld8(rsrc_t rs, unsigned off) { return __builtin_amdgcn_raw_buffer_load_b64(rs, (int)off, 0, 16); }
__device__ __forceinline__ v2u ld8s(rsrc_t rs, unsigned voff, unsigned soff) { return __builtin_amdgcn_raw_buffer_load_b64(rs, (int)voff, (int)soff, 16); }
__device__ __forceinline__ unsigned ld4(rsrc_t rs, unsigned off) { return __builtin_amdgcn_raw_buffer_load_b32(rs, (int)off, 0, 16); }
__device__ __forceinline__ v4u ld16p(rsrc_t rs, unsigned off) { return __builtin_amdgcn_raw_buffer_load_b128(rs, (int)off, 0, 0); }
__device__ __forceinline__ void st16(rsrc_t rs, unsigned off, v4u v) { __builtin_amdgcn_raw_buffer_store_b128(v, rs, (int)off, 0, 16); }
__device__ __forceinline__ void st16s(rsrc_t rs, unsigned voff, unsigned soff, v4u v) { __builtin_amdgcn_raw_buffer_store_b128(v, rs, (int)voff, (int)soff, 16); }
__device__ __forceinline__ void st8(rsrc_t rs, unsigned off, v2u v) { __builtin_amdgcn_raw_buffer_store_b64(v, rs, (int)off, 0, 16); }
__device__ __forceinline__ void st4(rsrc_t rs, unsigned off, unsigned v) { __builtin_amdgcn_raw_buffer_store_b32(v, rs, (int)off, 0, 16); }

__device__ __forceinline__ bool spin_fail(unsigned& spins, rsrc_t rs) {
  ++spins;
  if (spins > kSpinCap) { st4(rs, (unsigned)OFF_ABORT, 1u); return true; }
  if ((spins & 127u) == 0u) return __builtin_amdgcn_readfirstlane(ld4(rs, (unsigned)OFF_ABORT)) != 0u;
  return false;
}
template <int SLEEP>
__device__ __forceinline__ void wait_cnt(rsrc_t rs, unsigned off, int n, int probe, unsigned want, int lane) {
  unsigned spins = 0;
  for (;;) {
    const unsigned v = ld4(rs, off + (unsigned)probe * 128u);
    if (__builtin_amdgcn_readfirstlane(v) >= want) break;
    if (spin_fail(spins, rs)) break;
    __builtin_amdgcn_s_sleep(SLEEP);
  }
  if (n > 1) {
    for (;;) {
      const unsigned v = ld4(rs, off + (unsigned)(lane < n ? lane : 0) * 128u);
      if (__all(v >= want)) break;
      if (spin_fail(spins, rs)) break;
      __builtin_amdgcn_s_sleep(2);
    }
  }
  __builtin_amdgcn_fence(__ATOMIC_ACQUIRE, "wavefront");
}

__device__ __forceinline__ unsigned wait_table(rsrc_t rs, int c, int lane) {
  unsigned spins = 0, v;
  for (;;) {
    v = ld4(rs, cnt_off(K_COL, c, 0) + (unsigned)(lane & 15) * 128u);
    if (__all(v != 0u)) break;
    if (spin_fail(spins, rs)) break;
    __builtin_amdgcn_s_sleep(8);
  }
  return v - 1u;
}
__device__ __forceinline__ unsigned mem_copy(unsigned tab, unsigned x) {
  const unsigned long long m = __ballot(((tab >> 3) & 7u) == x) & 0xffffull;
  const int own = m == 0ull ? 0 : __builtin_ctzll(m);
  return (unsigned)own * (unsigned)CR_BLK + (unsigned)M_ROW0 + ((unsigned)__builtin_amdgcn_readlane(tab, own) & 7u) * 4096u;
}

__device__ __forceinline__ void layernorm8(const float (&x)[8], float (&y)[8]) {
  float s = 0.f;
#pragma unroll
  for (int j = 0; j < 8; ++j) s += x[j];
  const float mean = wave_sum(s) * (1.0f / DM);
  float q = 0.f;
#pragma unroll
  for (int j = 0; j < 8; ++j) { const float d = x[j] - mean; q += d * d; }
  const float var = wave_sum(q) * (1.0f / DM);
  const float m = bf16r(mean);
  const float rden = 1.0f / bf16r(sqrtf(bf16r(bf16r(var) + 1e-5f)));
#pragma unroll
  for (int j = 0; j < 8; ++j) y[j] = bf16r(x[j] - m) * rden;
}

typedef short v4s __attribute__((ext_vector_type(4)));
__device__ __forceinline__ v2u tr8(const LAS unsigned char* a) {
  return __builtin_bit_cast(v2u, __builtin_amdgcn_ds_read_tr16_b64_v4i16((LAS v4s*)a));
}

constexpr int NJ_QKV = 3 * 16 * 16;
constexpr int NJ_WO = 32 * 4 * 2;
constexpr int NJ_LN = S_TOK / 4;
constexpr int NJ_INIT = 256;
constexpr int NJ_ALL = NJ_QKV + NJ_WO + NJ_LN + NJ_INIT;

__global__ void __launch_bounds__(256) k_prep(PrepParams p) {
  __shared__ __attribute__((aligned(16))) unsigned char tile[32 * 528];
  const int tid = threadIdx.x, lane = tid & 63, w = tid >> 6;
  int job = blockIdx.x;
  if (job < NJ_QKV + NJ_WO) {
    const bool pi = job >= NJ_QKV;
    const u16* src; int ld; size_t dst, tstride;
    if (!pi) {
      const int m = job >> 8, ks = (job >> 4) & 15, hp = job & 15;
      src = (m == 0 ? p.wq : (m == 1 ? p.wk : p.wv)) + (size_t)(32 * ks) * (NH * DH) + hp * 256; ld = NH * DH;
      dst = OFF_WQKV + ((size_t)(m * 32 + 2 * hp) * 8 * 16 + ks) * KB; tstride = 16 * KB;
    } else {
      job -= NJ_QKV;
      const int h = job >> 3, ks = (job >> 1) & 3, nb = job & 1;
      src = p.wo + (size_t)(h * DH + 32 * ks) * DM + nb * 256; ld = DM;
      dst = OFF_WO + ((size_t)(h * 32 + nb * 16) * 4 + ks) * KB; tstride = 4 * KB;
    }
    v4u v[4];
#pragma unroll
    for (int it = 0; it < 4; ++it) v[it] = *(const v4u*)(src + (size_t)(it * 8 + (tid >> 5)) * ld + (tid & 31) * 8);
#pragma unroll
    for (int it = 0; it < 4; ++it) *(v4u*)(tile + (it * 8 + (tid >> 5)) * 528 + (tid & 31) * 16) = v[it];
    __syncthreads();
    const int g = lane >> 4, q = (lane >> 2) & 3, pc = lane & 3;
    const LAS unsigned char* T = (const LAS unsigned char*)tile;
#pragma unroll
    for (int it = 0; it < 4; ++it) {
      const int t = w * 4 + it;
      const LAS unsigned char* a = pi ? T + (4 * g + q) * 528 + (t >> 1) * 64 + pc * 16 + (t & 1) * 8 : T + (8 * g + q) * 528 + t * 32 + pc * 8;
      const v2u lo = tr8(a), hi = tr8(a + (pi ? 16 : 4) * 528);
      *(v4u*)(p.ws + dst + (size_t)t * tstride + lane * 16) = v4u{lo.x, lo.y, hi.x, hi.y};
    }
    return;
  }
  job -= NJ_QKV + NJ_WO;
  if (job < NJ_LN) {
    const int row = job * 4 + w;
    const v4u v = *(const v4u*)(p.x + (size_t)row * DM + lane * 8);
    float x[8] = {bflo(v.x), bfhi(v.x), bflo(v.y), bfhi(v.y), bflo(v.z), bfhi(v.z), bflo(v.w), bfhi(v.w)}, y[8];
    layernorm8(x, y);
    v4u o; o.x = pack2(y[0], y[1]); o.y = pack2(y[2], y[3]); o.z = pack2(y[4], y[5]); o.w = pack2(y[6], y[7]);
    *(v4u*)(p.ws + OFF_XA + (size_t)row * 1024 + lane * 16) = o;
    return;
  }
  job -= NJ_LN;
  {
    const v4u z = {0u, 0u, 0u, 0u};
    for (size_t i = (size_t)job * 256 + tid; i < ZERO_BYTES / 16; i += (size_t)NJ_INIT * 256) *(v4u*)(p.ws + i * 16) = z;
    *(v4u*)(p.ws + p_region(p.ws, 0) + (size_t)(job >> 3) * CR_BLK + (job & 7) * 4096 + tid * 16) = z;
  }
}

__device__ __forceinline__ int hkey(int row) { return ((row >> 1) & 1) | ((row >> 2) & 2); }
__device__ __forceinline__ void half_issue(const u16* src, int ld, int t, LAS unsigned char* img, int w, int lane) {
  const int row = lane >> 3, pos = (lane >> 1) & 3;
#pragma unroll
  for (int i = 0; i < 16; ++i) {
    const int r0 = (4 * i + w) * 8 + row;
    const u16* q = src + (size_t)r0 * ld + (2 * (pos ^ hkey(r0)) + t) * 16 + (lane & 1) * 8;
    const unsigned dst = (unsigned)(uintptr_t)img + (unsigned)((4 * i + w) * 1024);
    unsigned keep;
    asm volatile("s_mov_b32 %0, m0\n\ts_mov_b32 m0, %2\n\ts_nop 0\n\tglobal_load_lds_dwordx4 %1, off\n\ts_mov_b32 m0, %0"
                 : "=&s"(keep) : "v"(q), "s"(dst) : "memory");
  }
  SB();
}
__device__ __forceinline__ v4u half_frag(const LAS unsigned char* img, int w, int ks, int lane) {
  const int row = 8 * (lane >> 4) + ((lane >> 2) & 3);
  const LAS unsigned char* a = img + (32 * ks + row) * 128 + ((w ^ hkey(row)) * 32) + (lane & 3) * 8;
  const v2u lo = tr8(a), hi = tr8(a + 512);
  return v4u{lo.x, lo.y, hi.x, hi.y};
}
#define RAWBAR() do { asm volatile("s_waitcnt lgkmcnt(0)" ::: "memory"); __builtin_amdgcn_s_barrier(); asm volatile("" ::: "memory"); } while (0)
template <int NH2, class SRC, class USE>
__device__ __forceinline__ void stream_halves(LAS unsigned char* buf, int ld, int w, int lane, SRC&& src, USE&& use) {
  half_issue(src(0), ld, 0, buf, w, lane);
  half_issue(src(0), ld, 1, buf + 65536, w, lane);
#pragma unroll
  for (int i = 0; i < NH2; ++i) {
    if (i + 1 < NH2) asm volatile("s_waitcnt vmcnt(16)" ::: "memory");
    else asm volatile("s_waitcnt vmcnt(0)" ::: "memory");
    RAWBAR();
    use(i, buf + (i & 1) * 65536);
    RAWBAR();
    if (i + 2 < NH2) half_issue(src((i + 2) >> 1), ld, i & 1, buf + (i & 1) * 65536, w, lane);
  }
}
constexpr int PSTG = 16 * 272;
template <class OUT>
__device__ __forceinline__ void wave_panel(const u16* src, int ld, LAS unsigned char* stg, int lane, OUT&& out) {
  const u16* q = src + (size_t)(lane >> 4) * ld + (lane & 15) * 8;
  const LAS unsigned char* a = stg + (4 * (lane >> 4) + ((lane >> 2) & 3)) * 272 + (lane & 3) * 16;
#pragma unroll
  for (int bt = 0; bt < 2; ++bt) {
    v4u v[16];
#pragma unroll
    for (int i = 0; i < 16; ++i) v[i] = *(const v4u*)(q + (size_t)(64 * bt + 4 * i) * ld);
    SB();
#pragma unroll
    for (int k2 = 0; k2 < 2; ++k2) {
      v2u lo[8];
#pragma unroll
      for (int hf = 0; hf < 2; ++hf) {
#pragma unroll
        for (int i = 0; i < 4; ++i) *(LAS v4u*)(stg + ((lane >> 4) + 4 * i) * 272 + (lane & 15) * 16) = v[8 * k2 + 4 * hf + i];
#pragma unroll
        for (int t = 0; t < 8; ++t) {
          const v2u x = tr8(a + (t >> 1) * 64 + (t & 1) * 8);
          if (hf == 0) lo[t] = x;
          else out(t, 2 * bt + k2, v4u{lo[t].x, lo[t].y, x.x, x.y});
        }
        LDSWAIT();
        SB();
      }
    }
  }
}

constexpr float kScale = 0.08838834764831845f;
constexpr float kNegBig = -3.0e38f;
constexpr float kLog2e = 1.4426950408889634f;

__device__ __forceinline__ void softmax_frags(f32x4 sx0, f32x4 sx1, f32x4 sm, unsigned ok0, unsigned ok1, bool okm, v4u& p0, v4u& p1) {
  unsigned u[6] = {pack2(sx0.x, sx0.y), pack2(sx0.z, sx0.w), pack2(sx1.x, sx1.y), pack2(sx1.z, sx1.w), pack2(sm.x, sm.y), pack2(sm.z, sm.w)};
  float a[12];
#pragma unroll
  for (int j = 0; j < 6; ++j) {
    u[j] = pack2(bflo(u[j]) * kScale, bfhi(u[j]) * kScale);
    a[2 * j] = bflo(u[j]); a[2 * j + 1] = bfhi(u[j]);
  }
#pragma unroll
  for (int r = 0; r < 4; ++r) {
    a[r] = ((ok0 >> r) & 1u) ? a[r] : kNegBig;
    a[4 + r] = ((ok1 >> r) & 1u) ? a[4 + r] : kNegBig;
    a[8 + r] = okm ? a[8 + r] : kNegBig;
  }
  float mx = fmaxf(fmaxf(fmaxf(a[0], a[1]), fmaxf(a[2], a[3])), fmaxf(fmaxf(a[4], a[5]), fmaxf(a[6], a[7])));
  mx = fmaxf(mx, fmaxf(fmaxf(a[8], a[9]), fmaxf(a[10], a[11])));
  mx = xmax(mx);
  const float nm = -mx * kLog2e;
#pragma unroll
  for (int j = 0; j < 12; ++j) a[j] = __builtin_amdgcn_exp2f(fmaf(a[j], kLog2e, nm));
  float sum = ((a[0] + a[1]) + (a[2] + a[3])) + ((a[4] + a[5]) + (a[6] + a[7])) + ((a[8] + a[9]) + (a[10] + a[11]));
  sum = xsum(sum);
  const float inv = __builtin_amdgcn_rcpf(sum);
  p0.x = pack2(a[0] * inv, a[1] * inv); p0.y = pack2(a[2] * inv, a[3] * inv);
  p0.z = pack2(a[4] * inv, a[5] * inv); p0.w = pack2(a[6] * inv, a[7] * inv);
  p1.x = pack2(a[8] * inv, a[9] * inv); p1.y = pack2(a[10] * inv, a[11] * inv);
  p1.z = 0u; p1.w = 0u;
}

__device__ __forceinline__ void rows_load(rsrc_t rs, unsigned goff, v4u (&v)[8], int w, int lane) {
#pragma unroll
  for (int i = 0; i < 8; ++i) v[i] = ld16s(rs, (unsigned)lane * 16u + (unsigned)(i & 3) * 1024u, goff + (unsigned)(8 * w + (i & 4)) * 1024u);
  SB();
}
constexpr int RSTR = 1040;
constexpr int IMG = 32 * RSTR;
__device__ __forceinline__ void rows_put(LAS unsigned char* L, const v4u (&v)[8], int w, int lane) {
#pragma unroll
  for (int i = 0; i < 8; ++i) *(LAS v4u*)(L + (8 * w + i) * RSTR + lane * 16) = v[i];
}
__device__ __forceinline__ v4u row_frag(const LAS unsigned char* L, int tt, int ks, int fb) {
  return *(const LAS v4u*)(L + fb + ks * 64 + tt * 16 * RSTR);
}
__device__ __forceinline__ void partial_put(LAS unsigned char* L, const f32x4 (&acc)[8][2], int tt, int w, int lane) {
  LAS unsigned char* B = L + (16 * tt + (lane & 15)) * RSTR + w * 256 + (lane >> 4) * 16;
#pragma unroll
  for (int m = 0; m < 4; ++m) *(LAS v4u*)(B + m * 64) = pack8(acc[2 * m][tt], acc[2 * m + 1][tt]);
}
__device__ __forceinline__ v4u partial_row(const LAS unsigned char* L, int tt, int k, int w, int lane) {
  return *(const LAS v4u*)(L + (16 * tt + 4 * w + k) * RSTR + lane * 16);
}
__device__ __forceinline__ void partial_row_store(rsrc_t rs, unsigned goff, v4u v, int tt, int k, int w, int lane) {
  st16s(rs, (unsigned)lane * 16u + (unsigned)k * 1024u, goff + (unsigned)(16 * tt + 4 * w) * 1024u, v);
}
__device__ __forceinline__ void partial_store(rsrc_t rs, unsigned goff, const LAS unsigned char* L, int tt, int w, int lane) {
  v4u v[4];
#pragma unroll
  for (int k = 0; k < 4; ++k) v[k] = partial_row(L, tt, k, w, lane);
#pragma unroll
  for (int k = 0; k < 4; ++k) partial_row_store(rs, goff, v[k], tt, k, w, lane);
  SB();
}

constexpr int CH_KV = 0, CH_MEM = 16384, CH_EX = CH_MEM + 8192, CH_WV = CH_EX + 6144, CH_FL = CH_WV + 131072 + 2032;
__device__ __forceinline__ void role_chain(const Params& p, rsrc_t rs, LAS unsigned char* L, int c, int h) {
  const int tid = threadIdx.x, lane = tid & 63, w = __builtin_amdgcn_readfirstlane(tid >> 6);
  const int g = lane >> 4, r = lane & 15, r8 = lane & 7;
  v4u wq[2][16], wk[2][16], wo[8][4];
#pragma unroll
  for (int t = 0; t < 2; ++t)
#pragma unroll
    for (int ks = 0; ks < 16; ++ks) {
      const unsigned o = (unsigned)(((h * 8 + 2 * w + t) * 16 + ((4 * w + ks) & 15)) * 1024 + lane * 16);
      wq[t][ks] = ld16p(rs, (unsigned)OFF_WQKV + o);
      wk[t][ks] = ld16p(rs, (unsigned)(OFF_WQKV + 4 * MB) + o);
    }
  for (int i = 0; i < 32; ++i) {
    const int t = i >> 4, j = i & 15;
    *(LAS v4u*)(L + CH_WV + ((2 * w + t) * 16 + j) * 1024 + lane * 16) =
        ld16p(rs, (unsigned)(OFF_WQKV + 8 * MB) + (unsigned)(((h * 8 + 2 * w + t) * 16 + ((4 * w + j) & 15)) * 1024 + lane * 16));
  }
#pragma unroll
  for (int dt = 0; dt < 8; ++dt)
#pragma unroll
    for (int ks = 0; ks < 4; ++ks) wo[dt][ks] = ld16p(rs, (unsigned)OFF_WO + (unsigned)(((h * 32 + 8 * w + dt) * 4 + ks) * 1024 + lane * 16));
  v4u mo[4];
#pragma unroll
  for (int j = 0; j < 4; ++j) {
    mo[j] = *(const v4u*)(p.mem0 + r8 * DM + (4 * w + j) * 32 + g * 8);
    if (r < 8) *(LAS v4u*)(L + CH_MEM + (4 * w + j) * 512 + g * 128 + r * 16) = mo[j];
  }
  const unsigned qkv_base = (unsigned)(OFF_QKVF + (size_t)c * NSEG * NH * 24 * KB) + (unsigned)h * 24u * 1024u + 8192u;
  const unsigned kvm_base = (unsigned)(OFF_KVM + (size_t)c * NSEG * NH * 4 * KB) + (unsigned)h * 4096u;
  {
    wait_cnt<8>(rs, cnt_off(K_QKV, c, h), 1, 0, 1u, lane);
    v4u v[4];
#pragma unroll
    for (int i = 0; i < 4; ++i) v[i] = ld16(rs, qkv_base + (unsigned)((4 * w + i) * 1024 + lane * 16));
    SB();
#pragma unroll
    for (int i = 0; i < 4; ++i) *(LAS v4u*)(L + CH_KV + (4 * w + i) * 1024 + lane * 16) = v[i];
  }
  unsigned p_off[2], m_base;
  {
    const unsigned tab = wait_table(rs, c, lane);
#pragma unroll
    for (int q = 0; q < 2; ++q) {
      const unsigned ca = (unsigned)__builtin_amdgcn_readlane(tab, 4 * w + 2 * q) & 7u, cb = (unsigned)__builtin_amdgcn_readlane(tab, 4 * w + 2 * q + 1) & 7u;
      p_off[q] = (unsigned)((4 * w + 2 * q + (r >> 3)) * CR_BLK + g * 2 * ROW + h * 128 + r8 * 16) + (r < 8 ? ca : cb) * 4096u;
    }
    m_base = p_region(p.ws, c) + mem_copy(tab, xcc_id()) + (unsigned)((w >> 1) * ROW + (w & 1) * 2048 + g * 128 + r8 * 16);
  }
  unsigned have_qkv = 1;
  volatile LAS v4u* FL = (volatile LAS v4u*)(L + CH_FL);
  if (tid == 0) *FL = v4u{0u, 0u, 0u, 0u};
  __syncthreads();
  v4u wv0[2], wv1[2];
#pragma unroll
  for (int j = 0; j < 2; ++j) {
    wv0[j] = *(const LAS v4u*)(L + CH_WV + 2 * w * 16384 + lane * 16 + j * 1024);
    wv1[j] = *(const LAS v4u*)(L + CH_WV + 2 * w * 16384 + 16384 + lane * 16 + j * 1024);
  }
  for (int s = 0; s < NSEG; ++s) {
    const LAS unsigned char* MF = L + CH_MEM + g * 128 + r8 * 16;
    const LAS unsigned char* WV = L + CH_WV + 2 * w * 16384 + lane * 16;
    f32x4 aq0, aq1, ak0, ak1, av0, av1;
    v4u kx[8];
    {
      v4u fl = *FL;
      v4u mf[4];
      SB();
#define QKV_STEP(j, m) do { \
        MFA((j) == 0, aq0, wq[0][j], m); MFA((j) == 0, aq1, wq[1][j], m); SB(); \
        if ((j) >= 4 && (j) + 3 < 16) mf[((j) + 3) & 3] = *(const LAS v4u*)(MF + ((w + (((j) + 3) >> 2)) & 3) * 2048 + (((j) + 3) & 3) * 512); \
        MFA((j) == 0, ak0, wk[0][j], m); MFA((j) == 0, ak1, wk[1][j], m); SB(); \
        if ((j) >= 12) kx[(((j) - 12) & 1) + 4 * (((j) - 12) >> 1)] = *(const LAS v4u*)(L + CH_KV + ((((j) - 12) & 1) + 4 * (((j) - 12) >> 1)) * 1024 + lane * 16); \
        MFV((j) == 0, av0, m, wv0[(j) & 1]); SB(); \
        if ((j) + 2 < 16) wv0[(j) & 1] = *(const LAS v4u*)(WV + ((j) + 2) * 1024); \
        MFV((j) == 0, av1, m, wv1[(j) & 1]); SB(); \
        if ((j) + 2 < 16) wv1[(j) & 1] = *(const LAS v4u*)(WV + 16384 + ((j) + 2) * 1024); \
      } while (0)
      QKV_STEP(0, mo[0]);
      QKV_STEP(1, mo[1]);
      for (unsigned spins = 0; __builtin_amdgcn_readfirstlane(min(min(fl.x, fl.y), min(fl.z, fl.w))) < (unsigned)s && spins < (1u << 22); ++spins) fl = *FL;
#pragma unroll
      for (int i = 0; i < 3; ++i) mf[i] = *(const volatile LAS v4u*)(MF + ((w + 1) & 3) * 2048 + i * 512);
      SB();
      QKV_STEP(2, mo[2]);
      QKV_STEP(3, mo[3]);
#pragma unroll
      for (int j = 4; j < 16; ++j) { const v4u m = mf[j & 3]; QKV_STEP(j, m); }
#undef QKV_STEP
    }
    asm("s_nop 7\n\ts_nop 1" : "+v"(aq0), "+v"(aq1), "+v"(ak0), "+v"(ak1), "+v"(av0), "+v"(av1));
    {
      const v4u kq = r < 8 ? pack8(ak0, ak1) : pack8(aq0, aq1);
      *(LAS v4u*)(L + CH_EX + (r >> 3) * 4096 + w * 512 + g * 128 + r8 * 16) = kq;
      *(LAS v4u*)(L + CH_EX + 2048 + w * 512 + (g & 1) * 256 + r * 16) = pack8(av0, av1);
    }
    __syncthreads();
    const LAS unsigned char* EXL = L + CH_EX + g * 128 + r8 * 16;
    f32x4 sx0, sx1, sm;
    {
      v4u qf[4], km[2];
#pragma unroll
      for (int i = 0; i < 4; ++i) qf[i] = *(const LAS v4u*)(EXL + 4096 + i * 512);
#pragma unroll
      for (int i = 0; i < 2; ++i) km[i] = *(const LAS v4u*)(EXL + i * 512);
#pragma unroll
      for (int i = 2; i < 4; ++i) kx[i] = *(const LAS v4u*)(L + CH_KV + i * 1024 + lane * 16);
      SB();
#pragma unroll
      for (int i = 0; i < 2; ++i) {
        MFV(i == 0, sx0, kx[i], qf[i]); MFV(i == 0, sx1, kx[4 + i], qf[i]);
        SB();
        kx[6 + i] = *(const LAS v4u*)(L + CH_KV + (6 + i) * 1024 + lane * 16);
        MFV(i == 0, sm, km[i], qf[i]);
        SB();
        km[i] = *(const LAS v4u*)(EXL + (2 + i) * 512);
      }
#pragma unroll
      for (int i = 2; i < 4; ++i) MFV(false, sx0, kx[i], qf[i]);
#pragma unroll
      for (int i = 2; i < 4; ++i) { MFV(false, sx1, kx[4 + i], qf[i]); MFV(false, sm, km[i - 2], qf[i]); }
      SB();
    }
    v4u vx[4], vma[4];
#pragma unroll
    for (int et = 0; et < 4; ++et) vx[et] = *(const LAS v4u*)(L + CH_KV + 8192 + et * 1024 + lane * 16);
#pragma unroll
    for (int m = 0; m < 4; ++m) vma[m] = *(const LAS v4u*)(L + CH_EX + 2048 + m * 512 + (g & 1) * 256 + r * 16);
    SB();
    mfma_settle3(sx0, sx1, sm);
    v4u p0, p1;
    {
      const bool lo = r < 8;
      const f32x4 sa = lo ? sx0 : sx1;
      unsigned u0 = pack2(sa.x, sa.y), u1 = pack2(sa.z, sa.w), u2 = pack2(lo ? sm.x : sm.z, lo ? sm.y : sm.w);
      u0 = pack2(bflo(u0) * kScale, bfhi(u0) * kScale);
      u1 = pack2(bflo(u1) * kScale, bfhi(u1) * kScale);
      u2 = pack2(bflo(u2) * kScale, bfhi(u2) * kScale);
      float a0 = bflo(u0), a1 = bfhi(u0), a2 = bflo(u1), a3 = bfhi(u1);
      float a4 = g < 2 ? bflo(u2) : kNegBig, a5 = g < 2 ? bfhi(u2) : kNegBig;
      float mx = fmaxf(fmaxf(fmaxf(a0, a1), fmaxf(a2, a3)), fmaxf(a4, a5));
      mx = fmaxf(mx, ror8f(mx));
      mx = xmax(mx);
      const float nm = -mx * kLog2e;
      a0 = __builtin_amdgcn_exp2f(fmaf(a0, kLog2e, nm)); a1 = __builtin_amdgcn_exp2f(fmaf(a1, kLog2e, nm));
      a2 = __builtin_amdgcn_exp2f(fmaf(a2, kLog2e, nm)); a3 = __builtin_amdgcn_exp2f(fmaf(a3, kLog2e, nm));
      a4 = __builtin_amdgcn_exp2f(fmaf(a4, kLog2e, nm)); a5 = __builtin_amdgcn_exp2f(fmaf(a5, kLog2e, nm));
      float sum = ((a0 + a1) + (a2 + a3)) + (a4 + a5);
      sum += ror8f(sum);
      sum = xsum(sum);
      const float inv = __builtin_amdgcn_rcpf(sum);
      u0 = pack2(a0 * inv, a1 * inv); u1 = pack2(a2 * inv, a3 * inv); u2 = pack2(a4 * inv, a5 * inv);
      const unsigned o0 = ror8(u0), o1 = ror8(u1), o2 = ror8(u2);
      p0.x = lo ? u0 : o0; p0.y = lo ? u1 : o1; p0.z = lo ? o0 : u0; p0.w = lo ? o1 : u1;
      p1.x = lo ? u2 : o2; p1.y = lo ? o2 : u2; p1.z = 0u; p1.w = 0u;
    }
    const v4u p1h = {0u, 0u, p1.x, p1.y};
    if (s + 1 < NSEG) {
      v4u of[4];
      f32x4 o2[8], pa[8];
      unsigned pk[8][2];
      const unsigned soff = p_region(p.ws, c) + (unsigned)((s & 1) * ROW);
      mfma_pv(o2[0], o2[1], vx[0], vx[1], vma[0], p0, p1, p1h);
      SB();
      vx[0] = *(const LAS v4u*)(L + CH_KV + 8192 + 4 * 1024 + lane * 16);
      vx[1] = *(const LAS v4u*)(L + CH_KV + 8192 + 5 * 1024 + lane * 16);
      mfma_pv(o2[2], o2[3], vx[2], vx[3], vma[1], p0, p1, p1h);
      SB();
      vx[2] = *(const LAS v4u*)(L + CH_KV + 8192 + 6 * 1024 + lane * 16);
      vx[3] = *(const LAS v4u*)(L + CH_KV + 8192 + 7 * 1024 + lane * 16);
      mfma_pv(o2[4], o2[5], vx[0], vx[1], vma[2], p0, p1, p1h);
      SB();
      of[0] = pack8(o2[0], o2[1]);
      SB();
      mfma_pv(o2[6], o2[7], vx[2], vx[3], vma[3], p0, p1, p1h);
      SB();
      of[1] = pack8(o2[2], o2[3]);
      SB();
#pragma unroll
      for (int t = 0; t < 8; ++t) {
        MFV(true, pa[t], wo[t][0], of[0]);
        SB();
        if (t == 4) { of[2] = pack8(o2[4], o2[5]); SB(); }
      }
#pragma unroll
      for (int t = 0; t < 8; ++t) {
        MFV(false, pa[t], wo[t][1], of[1]);
        SB();
        if (t == 2) { of[3] = pack8(o2[6], o2[7]); SB(); }
      }
#define PACK_TILE(t) do { pk[t][0] = min(pack2(pa[t].x, pa[t].y), 0xfffffffeu); pk[t][1] = min(pack2(pa[t].z, pa[t].w), 0xfffffffeu); \
                          asm("" : "+v"(pk[t][0]), "+v"(pk[t][1])); SB(); } while (0)
#pragma unroll
      for (int t = 0; t < 8; ++t) {
        MFV(false, pa[t], wo[t][2], of[2]);
        MFV(false, pa[t], wo[t][3], of[3]);
        SB();
        if (t >= 4) PACK_TILE(t - 4);
      }
      {
        const v4u o = {r < 8 ? pk[0][0] : pk[2][0], r < 8 ? pk[0][1] : pk[2][1], r < 8 ? pk[1][0] : pk[3][0], r < 8 ? pk[1][1] : pk[3][1]};
        st16s(rs, p_off[0], soff, o);
        SB();
      }
      PACK_TILE(4);
      PACK_TILE(5);
      asm("s_nop 1" : "+v"(pa[6]), "+v"(pa[7]));
      PACK_TILE(6);
      PACK_TILE(7);
#undef PACK_TILE
      const v4u o = {r < 8 ? pk[4][0] : pk[6][0], r < 8 ? pk[4][1] : pk[6][1], r < 8 ? pk[5][0] : pk[7][0], r < 8 ? pk[5][1] : pk[7][1]};
      st16s(rs, p_off[1], soff, o);
      SB();
    }
    st16(rs, kvm_base + (unsigned)(s * NH * 4096 + w * 1024 + lane * 16), *(const LAS v4u*)(L + CH_EX + w * 1024 + lane * 16));
    __syncthreads();
    const bool more = s + 1 < NSEG;
    const bool kv_known = more && have_qkv >= (unsigned)(s + 2);
    if (kv_known) {
      v4u v[4];
#pragma unroll
      for (int i = 0; i < 4; ++i) v[i] = ld16s(rs, (unsigned)(i * 1024 + lane * 16), qkv_base + (unsigned)((s + 1) * NH * 24 * 1024 + 4 * w * 1024));
      SB();
#pragma unroll
      for (int i = 0; i < 4; ++i) *(LAS v4u*)(L + CH_KV + (4 * w + i) * 1024 + lane * 16) = v[i];
    }
    DRAIN();
    __syncthreads();
    if (tid == 0) st4(rs, cnt_off(K_KM, c, h), (unsigned)(s + 1));
    if (!more) break;
    {
      if (!kv_known) {
        unsigned spins = 0;
        for (;;) {
          have_qkv = __builtin_amdgcn_readfirstlane(ld4(rs, cnt_off(K_QKV, c, h)));
          if (have_qkv >= (unsigned)(s + 2)) break;
          if (spin_fail(spins, rs)) break;
          __builtin_amdgcn_s_sleep(4);
        }
        __builtin_amdgcn_fence(__ATOMIC_ACQUIRE, "wavefront");
        v4u v[4];
#pragma unroll
        for (int i = 0; i < 4; ++i) v[i] = ld16s(rs, (unsigned)(i * 1024 + lane * 16), qkv_base + (unsigned)((s + 1) * NH * 24 * 1024 + 4 * w * 1024));
        SB();
#pragma unroll
        for (int i = 0; i < 4; ++i) *(LAS v4u*)(L + CH_KV + (4 * w + i) * 1024 + lane * 16) = v[i];
      } else if (have_qkv < (unsigned)(s + 3)) {
        have_qkv = __builtin_amdgcn_readfirstlane(ld4(rs, cnt_off(K_QKV, c, h)));
        __builtin_amdgcn_fence(__ATOMIC_ACQUIRE, "wavefront");
      }
#pragma unroll
      for (int j = 0; j < 2; ++j) {
        wv0[j] = *(const LAS v4u*)(L + CH_WV + 2 * w * 16384 + lane * 16 + j * 1024);
        wv1[j] = *(const LAS v4u*)(L + CH_WV + 2 * w * 16384 + 16384 + lane * 16 + j * 1024);
      }
      const unsigned slot = (unsigned)(((s + 1) % 3) * M_SLOT);
      unsigned spins = 0;
      for (;;) {
#pragma unroll
        for (int j = 0; j < 4; ++j) mo[j] = ld16s(rs, m_base + (unsigned)(j * 512), slot);
        SB();
        unsigned mx = 0;
#pragma unroll
        for (int j = 0; j < 4; ++j) mx = max(mx, max(max(mo[j].x, mo[j].y), max(mo[j].z, mo[j].w)));
        if (__all(mx != 0xffffffffu)) break;
        if (spin_fail(spins, rs)) break;
      }
      if (r < 8) {
#pragma unroll
        for (int j = 0; j < 4; ++j) *(LAS v4u*)(L + CH_MEM + (4 * w + j) * 512 + g * 128 + r * 16) = mo[j];
      }
      LDSWAIT();
      if (lane == 0) ((volatile LAS unsigned*)FL)[w] = (unsigned)(s + 1);
      asm volatile("" ::: "memory");
    }
  }
}

__device__ __forceinline__ void role_creduce(const Params& p, rsrc_t rs, LAS unsigned char* L, int c, int idx) {
  const int tid = threadIdx.x, lane = tid & 63, w = __builtin_amdgcn_readfirstlane(tid >> 6);
  const unsigned l16 = (unsigned)lane * 16u;
  const unsigned xcc = xcc_id();
  const unsigned blk = p_region(p.ws, c) + (unsigned)(idx * CR_BLK);
  unsigned col;
  {
    volatile LAS unsigned* T = (volatile LAS unsigned*)L;
    if (w == 0) {
      for (int i = 0; i < (c == 0 ? 0 : 4); ++i) __builtin_amdgcn_s_sleep(127);
      unsigned best = 0xffffffffu, arg = 0;
#pragma unroll 1
      for (int q = 0; q < 8 * (c == 0 ? CAL_N : 3); ++q) {
        const unsigned t0 = (unsigned)__builtin_amdgcn_s_memtime();
        unsigned y = ld4(rs, blk + (unsigned)((q & 7) * 4096 + (q >> 3) * 1024));
        asm volatile("s_waitcnt vmcnt(0)" : "+v"(y) :: "memory");
        const unsigned t1 = (unsigned)__builtin_amdgcn_s_memtime();
        if (t1 - t0 < best) { best = t1 - t0; arg = (unsigned)q & 7u; }
      }
      if (lane == 0) T[0] = arg;
    }
    __syncthreads();
    col = __builtin_amdgcn_readfirstlane(T[0]);
    const v4u o = {~0u, ~0u, ~0u, ~0u};
#pragma unroll
    for (int k = 0; k < 8; ++k) st16s(rs, l16 + (unsigned)(k & 3) * 1024u, blk + (unsigned)((2 * w + (k >> 2)) * ROW) + col * 4096u, o);
#pragma unroll
    for (int k = 0; k < 6; ++k) st16s(rs, l16 + (unsigned)w * 1024u, blk + (unsigned)(M_ROW0 + k * ROW) + col * 4096u, o);
    DRAIN();
    __syncthreads();
    if (tid == 0) st4(rs, cnt_off(K_COL, c, idx), 1u + col + 8u * xcc);
  }
  const unsigned p_base = blk + (unsigned)(2 * w * ROW) + col * 4096u;
  v4u sel;
  {
    const unsigned one = (lane & 1) ? 0x3f800000u : 0x00003f80u, q = ((unsigned)lane & 7u) >> 1;
    sel.x = q == 0u ? one : 0u; sel.y = q == 1u ? one : 0u; sel.z = q == 2u ? one : 0u; sel.w = q == 3u ? one : 0u;
  }
  unsigned m_off0, m_off1;
  {
    const unsigned tab = wait_table(rs, c, lane);
    const unsigned x0 = 2u * ((unsigned)lane >> 5) + (((unsigned)lane >> 3) & 1u);
    unsigned c0 = 0, c1 = 0;
#pragma unroll
    for (unsigned x = 0; x < 4; ++x) {
      const unsigned a = mem_copy(tab, x), b = mem_copy(tab, x + 4u);
      c0 = x0 == x ? a : c0; c1 = x0 == x ? b : c1;
    }
    const unsigned m_in = p_region(p.ws, c) + (unsigned)((idx >> 3) * ROW + (idx & 7) * 512 + w * 128 + (lane & 7) * 16 + ((lane >> 4) & 1) * 8);
    m_off0 = m_in + c0;
    m_off1 = m_in + c1;
  }
  for (int s = 0; s + 1 < NSEG; ++s) {
    const unsigned pb = p_base + (unsigned)((s & 1) * ROW);
    v4u x[4]; unsigned spins = 0;
    if (s > 0) __builtin_amdgcn_s_sleep(CR_SLEEP);
    for (;;) {
#pragma unroll
      for (int k = 0; k < 4; ++k) x[k] = ld16s(rs, l16 + (unsigned)k * 1024u, pb);
      SB();
      unsigned mx = 0;
#pragma unroll
      for (int k = 0; k < 4; ++k) mx = max(mx, max(max(x[k].x, x[k].y), max(x[k].z, x[k].w)));
      if (__all(mx != 0xffffffffu)) break;
      if (spin_fail(spins, rs)) break;
    }
    f32x4 a, a2;
    mfma_k2(a, sel, x[0], x[1]);
    mfma_k2(a2, sel, x[2], x[3]);
    mfma_settle2(a, a2);
    a += a2;
    a.x += ror8f(a.x); a.y += ror8f(a.y); a.z += ror8f(a.z); a.w += ror8f(a.w);
    v2u o; o.x = min(pack2(a.x, a.y), 0xfffffffeu); o.y = min(pack2(a.z, a.w), 0xfffffffeu);
    const unsigned slot = (unsigned)(((s + 1) % 3) * M_SLOT);
    st8(rs, m_off0 + slot, o);
    st8(rs, m_off1 + slot, o);
    __builtin_amdgcn_s_sleep(16);
    const v4u ones4 = {~0u, ~0u, ~0u, ~0u};
#pragma unroll
    for (int k = 0; k < 4; ++k) st16s(rs, l16 + (unsigned)k * 1024u, pb, ones4);
    const unsigned old = (unsigned)((s % 3) * M_SLOT);
    const v2u ones = {~0u, ~0u};
    st8(rs, m_off0 + old, ones);
    st8(rs, m_off1 + old, ones);
  }
}

__device__ __forceinline__ void role_qkv(const Params& p, rsrc_t rs, LAS unsigned char* L, int c, int h) {
  const int tid = threadIdx.x, lane = tid & 63, w = __builtin_amdgcn_readfirstlane(tid >> 6);
  const int g = lane >> 4, r = lane & 15;
  v4u wq[2][16], wk[2][16], wv[2][16];
#pragma unroll
  for (int t = 0; t < 2; ++t)
#pragma unroll
    for (int ks = 0; ks < 16; ++ks) {
      const unsigned o = (unsigned)(((h * 8 + 2 * w + t) * 16 + ks) * 1024 + lane * 16);
      wq[t][ks] = ld16p(rs, (unsigned)OFF_WQKV + o);
      wk[t][ks] = ld16p(rs, (unsigned)(OFF_WQKV + 4 * MB) + o);
      wv[t][ks] = ld16p(rs, (unsigned)(OFF_WQKV + 8 * MB) + o);
    }
  const int fb = r * RSTR + g * 16;
  volatile LAS unsigned* RDY = (volatile LAS unsigned*)(L + 2 * IMG);
  const unsigned xbase = (unsigned)(OFF_XA + (size_t)c * 2 * MB);
  bool staged = false;
  for (int s = 0; s < NSEG; ++s) {
    LAS unsigned char* X = L + (s & 1) * IMG;
    if (!staged) {
      if (c == 1) wait_cnt<POLL>(rs, cnt_off(K_X2, 0, 8 * w), 8, h & 7, (unsigned)(s + 1), lane);
      v4u v[8];
      rows_load(rs, xbase + (unsigned)s * 32768u, v, w, lane);
      rows_put(X, v, w, lane);
      __syncthreads();
    }
    unsigned nx = 0xffffffffu;
    if (c == 1 && w == 0) nx = ld4(rs, cnt_off(K_X2, 0, 0) + (unsigned)(lane & 31) * 128u);
    v4u nv[8];
    if (c == 0 && s + 1 < NSEG) rows_load(rs, xbase + (unsigned)(s + 1) * 32768u, nv, w, lane);
    f32x4 aq[2][2], ak[2][2], av[2][2];
    {
      v4u xf[4];
#pragma unroll
      for (int i = 0; i < 4; ++i) xf[i] = row_frag(X, i & 1, i >> 1, fb);
      SB();
#pragma unroll
      for (int i = 0; i < 32; ++i) {
        const int ks = i >> 1, tt = i & 1;
        const v4u x = xf[i & 3];
        MFA(ks == 0, aq[0][tt], wq[0][ks], x);
        MFA(ks == 0, aq[1][tt], wq[1][ks], x);
        MFA(ks == 0, ak[0][tt], wk[0][ks], x);
        SB();
        if (i >= 1 && i + 3 < 32) xf[(i + 3) & 3] = row_frag(X, (i + 3) & 1, (i + 3) >> 1, fb);
        MFA(ks == 0, ak[1][tt], wk[1][ks], x);
        MFV(ks == 0, av[0][tt], x, wv[0][ks]);
        MFV(ks == 0, av[1][tt], x, wv[1][ks]);
        SB();
      }
    }
    mfma_settle(av[0][0], av[1][0], av[0][1], av[1][1]);
    mfma_settle(aq[0][0], aq[1][0], ak[0][0], ak[1][0]);
    mfma_settle(aq[0][1], aq[1][1], ak[0][1], ak[1][1]);
    const unsigned ob = (unsigned)(OFF_QKVF + (size_t)((c * NSEG + s) * NH + h) * 24 * KB) + (unsigned)lane * 16u;
#pragma unroll
    for (int tt = 0; tt < 2; ++tt) {
      st16(rs, ob + (unsigned)(8192 + (tt * 4 + w) * 1024), pack8(ak[0][tt], ak[1][tt]));
      st16(rs, ob + (unsigned)((tt * 4 + w) * 1024), pack8(aq[0][tt], aq[1][tt]));
    }
#pragma unroll
    for (int t = 0; t < 2; ++t) st16(rs, ob + (unsigned)(16384 + (2 * w + t) * 1024), pack8(av[t][0], av[t][1]));
    if (c == 0) {
      if (s + 1 < NSEG) rows_put(L + ((s + 1) & 1) * IMG, nv, w, lane);
      staged = true;
    } else if (w == 0) {
      const bool ok = __all(nx >= (unsigned)(s + 2)) && s + 1 < NSEG;
      if (lane == 0) RDY[0] = ok ? 1u : 0u;
    }
    DRAIN();
    __syncthreads();
    if (tid == 0) st4(rs, cnt_off(K_QKV, c, h), (unsigned)(s + 1));
    if (c == 1) {
      staged = RDY[0] != 0u;
      if (staged) {
        v4u v[8];
        rows_load(rs, xbase + (unsigned)(s + 1) * 32768u, v, w, lane);
        rows_put(L + ((s + 1) & 1) * IMG, v, w, lane);
        __syncthreads();
      }
    }
  }
}

__device__ __forceinline__ void role_att(const Params& p, rsrc_t rs, LAS unsigned char* L, int c, int pp) {
  const int tid = threadIdx.x, lane = tid & 63, w = __builtin_amdgcn_readfirstlane(tid >> 6);
  const int g = lane >> 4, r = lane & 15, r8 = lane & 7;
  v4u wo[2][8][4];
#pragma unroll
  for (int hh = 0; hh < 2; ++hh)
#pragma unroll
    for (int dt = 0; dt < 8; ++dt)
#pragma unroll
      for (int ks = 0; ks < 4; ++ks)
        wo[hh][dt][ks] = ld16p(rs, (unsigned)OFF_WO + (unsigned)((((2 * pp + hh) * 32 + 8 * w + dt) * 4 + ks) * 1024 + lane * 16));
  const int hh = w >> 1, tt = w & 1, h = 2 * pp + hh;
  const int q = 16 * tt + r;
  unsigned ok0 = 0, ok1 = 0;
#pragma unroll
  for (int j = 0; j < 4; ++j) { ok0 |= (4 * g + j <= q ? 1u : 0u) << j; ok1 |= (16 + 4 * g + j <= q ? 1u : 0u) << j; }
  LAS unsigned char* EX = L;
  LAS unsigned char* TR = L + 16384;
  const unsigned qbase = (unsigned)(OFF_QKVF + (size_t)(c * NSEG * NH + h) * 24 * KB);
  const unsigned mbase = (unsigned)(OFF_KVM + (size_t)(c * NSEG * NH + h) * 4 * KB);
  const unsigned l16 = (unsigned)lane * 16u;
  unsigned have_qkv = 0, have_km = 0;
  v4u qf[4], kf[8], vf[8];
  bool loaded = false;
  for (int s = 0; s < NSEG; ++s) {
    if (!loaded) {
      if (have_qkv < (unsigned)(s + 1)) wait_cnt<8>(rs, cnt_off(K_QKV, c, h), 1, 0, (unsigned)(s + 1), lane);
      const unsigned ib = qbase + (unsigned)(s * NH * 24 * 1024);
#pragma unroll
      for (int ks = 0; ks < 4; ++ks) qf[ks] = ld16s(rs, l16 + (unsigned)(ks * 1024), ib + (unsigned)(tt * 4096));
#pragma unroll
      for (int i = 0; i < 8; ++i) kf[i] = ld16s(rs, l16 + (unsigned)((i & 3) * 1024), ib + (unsigned)(8192 + (i & 4) * 1024));
#pragma unroll
      for (int i = 0; i < 8; ++i) vf[i] = ld16s(rs, l16 + (unsigned)((i & 3) * 1024), ib + (unsigned)(16384 + (i & 4) * 1024));
      SB();
    }
    f32x4 sx0, sx1, sm;
#pragma unroll
    for (int ks = 0; ks < 4; ++ks) { MFV(ks == 0, sx0, kf[ks], qf[ks]); MFV(ks == 0, sx1, kf[4 + ks], qf[ks]); }
    SB();
    if (have_km < (unsigned)(s + 1)) wait_cnt<POLL>(rs, cnt_off(K_KM, c, h), 1, 0, (unsigned)(s + 1), lane);
    const unsigned mb = mbase + (unsigned)(s * NH * 4096);
    v4u km[4], vma[4];
#pragma unroll
    for (int ks = 0; ks < 4; ++ks) km[ks] = ld16s(rs, (unsigned)(ks * 512 + g * 128 + r8 * 16), mb);
#pragma unroll
    for (int m = 0; m < 4; ++m) vma[m] = ld16s(rs, (unsigned)(2048 + m * 512 + (g & 1) * 256 + r * 16), mb);
    const unsigned nq = ld4(rs, cnt_off(K_QKV, c, h));
    SB();
#pragma unroll
    for (int ks = 0; ks < 4; ++ks) MFV(ks == 0, sm, km[ks], qf[ks]);
    mfma_settle3(sx0, sx1, sm);
    v4u p0, p1;
    softmax_frags(sx0, sx1, sm, ok0, ok1, g < 2, p0, p1);
    const v4u p1h = {0u, 0u, p1.x, p1.y};
    {
      f32x4 o2[8];
#pragma unroll
      for (int m = 0; m < 4; ++m)
        mfma_pv(o2[2 * m], o2[2 * m + 1], vf[2 * m], vf[2 * m + 1], vma[m], p0, p1, p1h);
      mfma_settle(o2[0], o2[1], o2[2], o2[3]);
      mfma_settle(o2[4], o2[5], o2[6], o2[7]);
#pragma unroll
      for (int m = 0; m < 4; ++m) *(LAS v4u*)(EX + (w * 4 + m) * 1024 + lane * 16) = pack8(o2[2 * m], o2[2 * m + 1]);
    }
    have_qkv = __builtin_amdgcn_readfirstlane(nq);
    loaded = s + 1 < NSEG && have_qkv >= (unsigned)(s + 2);
    __syncthreads();
    f32x4 acc[8][2];
    const unsigned pout = (unsigned)(OFF_PO + (size_t)(s * NATT + pp) * 32 * KB);
    {
      const unsigned ib = qbase + (unsigned)((loaded ? s + 1 : s) * NH * 24 * 1024);
      v4u of[4], rw;
#define OFRAG(i) ((((i) >> 2) & 1) * 8 + ((i) >> 3) * 4 + ((i) & 3))
#pragma unroll
      for (int i = 0; i < 4; ++i) of[i] = *(const LAS v4u*)(EX + OFRAG(i) * 1024 + lane * 16);
      SB();
#pragma unroll
      for (int i = 0; i < 16; ++i) {
        const int t2 = i >> 3, h2 = (i >> 2) & 1, ks = i & 3;
        const bool first = h2 == 0 && ks == 0;
        const v4u x = of[i & 3];
#pragma unroll
        for (int dt = 0; dt < 4; ++dt) MFA(first, acc[dt][t2], wo[h2][dt][ks], x);
        SB();
        if (i >= 1 && i + 3 < 16) of[(i + 3) & 3] = *(const LAS v4u*)(EX + OFRAG(i + 3) * 1024 + lane * 16);
        if (i == 9) partial_put(TR, acc, 0, w, lane);
        if (i >= 11 && i < 15) rw = partial_row(TR, 0, i - 11, w, lane);
        MFA(first, acc[4][t2], wo[h2][4][ks], x);
        MFA(first, acc[5][t2], wo[h2][5][ks], x);
        SB();
        if (i < 4) qf[i] = ld16s(rs, l16 + (unsigned)(i * 1024), ib + (unsigned)(tt * 4096));
        else if (i < 12) kf[i - 4] = ld16s(rs, l16 + (unsigned)(((i - 4) & 3) * 1024), ib + (unsigned)(8192 + ((i - 4) & 4) * 1024));
        else vf[i - 12] = ld16s(rs, l16 + (unsigned)((i - 12) * 1024), ib + 16384u);
        MFA(first, acc[6][t2], wo[h2][6][ks], x);
        SB();
        if (i >= 12) vf[i - 8] = ld16s(rs, l16 + (unsigned)((i - 12) * 1024), ib + 20480u);
        if (i >= 11 && i < 15) partial_row_store(rs, pout, rw, 0, i - 11, w, lane);
        MFA(first, acc[7][t2], wo[h2][7][ks], x);
        SB();
        if (i == 10) __syncthreads();
      }
#undef OFRAG
    }
    mfma_settle(acc[0][1], acc[1][1], acc[2][1], acc[3][1]);
    mfma_settle(acc[4][1], acc[5][1], acc[6][1], acc[7][1]);
    partial_put(TR, acc, 1, w, lane);
    __syncthreads();
    partial_store(rs, pout, TR, 1, w, lane);
    const unsigned nk = ld4(rs, cnt_off(K_KM, c, h));
    DRAIN();
    have_km = __builtin_amdgcn_readfirstlane(nk);
    __syncthreads();
    if (tid == 0) st4(rs, cnt_off(K_ATT, c, pp), (unsigned)(s + 1));
  }
}

__device__ __forceinline__ float gelu_tanh(float x) {
  const float z = x * fmaf(x * x, -2.0f * 0.7978845608028654f * 0.044715f * 1.4426950408889634f, -2.0f * 0.7978845608028654f * 1.4426950408889634f);
  return x * __builtin_amdgcn_rcpf(1.0f + __builtin_amdgcn_exp2f(z));
}
__device__ __forceinline__ unsigned geglu2(float g0, float g1, float u0, float u1) {
  const unsigned gp = pack2(g0, g1), up = pack2(u0, u1);
  const unsigned ap = pack2(gelu_tanh(bflo(gp)), gelu_tanh(bfhi(gp)));
  return pack2(bflo(ap) * bflo(up), bfhi(ap) * bfhi(up));
}
__device__ __forceinline__ void role_ffn(const Params& p, rsrc_t rs, LAS unsigned char* L, int c, int sl) {
  const int tid = threadIdx.x, lane = tid & 63, w = __builtin_amdgcn_readfirstlane(tid >> 6);
  const int g = lane >> 4, r = lane & 15;
  v4u wg[2][16], wu[2][16], wd[8][4];
  stream_halves<4>(L, DFF, w, lane,
      [&](int m) { return (m == 0 ? p.wg : p.wu) + sl * 128; },
      [&](int i, LAS unsigned char* img) {
#pragma unroll
        for (int ks = 0; ks < 16; ++ks) {
          const v4u f = half_frag(img, w, ks, lane);
          if (i < 2) wg[i & 1][ks] = f; else wu[i & 1][ks] = f;
        }
      });
  wave_panel(p.wd + (size_t)sl * 128 * DM + w * 128, DM, L + w * PSTG, lane, [&](int t, int ks, v4u f) { wd[t][ks] = f; });
  __syncthreads();
  LAS unsigned char* X = L;
  LAS unsigned char* EX = L + IMG;
  LAS unsigned char* TR = L + IMG + 8192;
  const int fb = r * RSTR + g * 16;
  volatile LAS unsigned* RDY = (volatile LAS unsigned*)(L + 2 * IMG + 8192);
  const unsigned xbase = (unsigned)(OFF_XF + (size_t)c * 2 * MB);
  bool staged = false;
  for (int s = 0; s < NSEG; ++s) {
    if (!staged) {
      wait_cnt<POLL>(rs, cnt_off(K_X1, c, 8 * w), 8, sl & 7, (unsigned)(s + 1), lane);
      v4u v[8];
      rows_load(rs, xbase + (unsigned)s * 32768u, v, w, lane);
      rows_put(X, v, w, lane);
      __syncthreads();
    }
    f32x4 ag[2][2], au[2][2];
    {
      v4u xf[4];
#pragma unroll
      for (int i = 0; i < 4; ++i) xf[i] = row_frag(X, 0, i, fb);
      SB();
      unsigned hv[4];
#pragma unroll
      for (int i = 0; i < 32; ++i) {
        const int ks = i & 15, tt = i >> 4;
        const v4u x = xf[i & 3];
        MFA(ks == 0, ag[0][tt], wg[0][ks], x);
        MFA(ks == 0, ag[1][tt], wg[1][ks], x);
        SB();
        if (i >= 1 && i + 3 < 32) xf[(i + 3) & 3] = row_frag(X, (i + 3) >> 4, (i + 3) & 15, fb);
        MFA(ks == 0, au[0][tt], wu[0][ks], x);
        MFA(ks == 0, au[1][tt], wu[1][ks], x);
        SB();
        if (i >= 19 && i < 27 && (i & 1)) {
          const int q = (i - 19) >> 1, t = q >> 1, j = (q & 1) * 2;
          hv[q] = geglu2(ag[t][0][j], ag[t][0][j + 1], au[t][0][j], au[t][0][j + 1]);
          SB();
        }
        if (i == 27) { *(LAS v4u*)(EX + w * 1024 + lane * 16) = v4u{hv[0], hv[1], hv[2], hv[3]}; SB(); }
      }
    }
    __syncthreads();
    const unsigned pout = (unsigned)(OFF_PF + (size_t)(s * NFFN + sl) * 32 * KB);
    f32x4 acc[8][2];
    unsigned nx = 0;
    {
      v4u hf[4];
#pragma unroll
      for (int i = 0; i < 4; ++i) hf[i] = *(const LAS v4u*)(EX + i * 1024 + lane * 16);
      mfma_settle(ag[0][1], ag[1][1], au[0][1], au[1][1]);
      SB();
      unsigned hv[4];
#pragma unroll
      for (int ks = 0; ks < 4; ++ks) {
        const int t = ks >> 1, j = (ks & 1) * 2;
        const unsigned gp = pack2(ag[t][1][j], ag[t][1][j + 1]), up = pack2(au[t][1][j], au[t][1][j + 1]);
        MFV(ks == 0, acc[0][0], wd[0][ks], hf[ks]); MFV(ks == 0, acc[1][0], wd[1][ks], hf[ks]);
        SB();
        const float g0 = gelu_tanh(bflo(gp));
        MFV(ks == 0, acc[2][0], wd[2][ks], hf[ks]); MFV(ks == 0, acc[3][0], wd[3][ks], hf[ks]);
        SB();
        const float g1 = gelu_tanh(bfhi(gp));
        MFV(ks == 0, acc[4][0], wd[4][ks], hf[ks]); MFV(ks == 0, acc[5][0], wd[5][ks], hf[ks]);
        SB();
        const unsigned ap = pack2(g0, g1);
        hv[ks] = pack2(bflo(ap) * bflo(up), bfhi(ap) * bfhi(up));
        MFV(ks == 0, acc[6][0], wd[6][ks], hf[ks]); MFV(ks == 0, acc[7][0], wd[7][ks], hf[ks]);
        SB();
      }
      *(LAS v4u*)(EX + (4 + w) * 1024 + lane * 16) = v4u{hv[0], hv[1], hv[2], hv[3]};
      mfma_settle(acc[0][0], acc[1][0], acc[2][0], acc[3][0]);
      mfma_settle(acc[4][0], acc[5][0], acc[6][0], acc[7][0]);
      partial_put(TR, acc, 0, w, lane);
      if (w == 0) nx = ld4(rs, cnt_off(K_X1, c, 0) + (unsigned)(lane & 31) * 128u);
      __syncthreads();
#pragma unroll
      for (int i = 0; i < 4; ++i) hf[i] = *(const LAS v4u*)(EX + (4 + i) * 1024 + lane * 16);
      v4u rw = partial_row(TR, 0, 0, w, lane);
      SB();
#pragma unroll
      for (int ks = 0; ks < 4; ++ks) {
#pragma unroll
        for (int dt = 0; dt < 4; ++dt) MFV(ks == 0, acc[dt][1], wd[dt][ks], hf[ks]);
        SB();
        partial_row_store(rs, pout, rw, 0, ks, w, lane);
        MFV(ks == 0, acc[4][1], wd[4][ks], hf[ks]); MFV(ks == 0, acc[5][1], wd[5][ks], hf[ks]);
        SB();
        if (ks < 3) rw = partial_row(TR, 0, ks + 1, w, lane);
        MFV(ks == 0, acc[6][1], wd[6][ks], hf[ks]); MFV(ks == 0, acc[7][1], wd[7][ks], hf[ks]);
        SB();
      }
    }
    mfma_settle(acc[0][1], acc[1][1], acc[2][1], acc[3][1]);
    mfma_settle(acc[4][1], acc[5][1], acc[6][1], acc[7][1]);
    partial_put(TR, acc, 1, w, lane);
    if (w == 0) {
      const bool ok = __all(nx >= (unsigned)(s + 2)) && s + 1 < NSEG;
      if (lane == 0) RDY[0] = ok ? 1u : 0u;
    }
    __syncthreads();
    staged = RDY[0] != 0u;
    if (staged) {
      v4u nv[8];
      partial_store(rs, pout, TR, 1, w, lane);
      rows_load(rs, xbase + (unsigned)(s + 1) * 32768u, nv, w, lane);
      rows_put(X, nv, w, lane);
      DRAIN();
      __syncthreads();
      if (tid == 0) st4(rs, cnt_off(K_FFN, c, sl), (unsigned)(s + 1));
    } else {
      partial_store(rs, pout, TR, 1, w, lane);
      DRAIN();
      __syncthreads();
      if (tid == 0) st4(rs, cnt_off(K_FFN, c, sl), (unsigned)(s + 1));
    }
  }
}

template <int NP>
__device__ __forceinline__ void sum_rows(rsrc_t rs, unsigned off, float (&a)[8]) {
  v4u v[NP];
#pragma unroll
  for (int i = 0; i < NP; ++i) v[i] = ld16(rs, off + (unsigned)i * 32768u);
  SB();
#pragma unroll
  for (int j = 0; j < 8; ++j) a[j] = 0.f;
  unsigned c0 = 0x00003f80u, c1 = 0x3f800000u;
  asm("" : "+v"(c0), "+v"(c1));
  const bf16x2 e0 = __builtin_bit_cast(bf16x2, c0), e1 = __builtin_bit_cast(bf16x2, c1);
#pragma unroll
  for (int i = 0; i < NP; ++i) {
#pragma unroll
    for (int q = 0; q < 4; ++q) {
      const unsigned xu = v[i][q];
      const bf16x2 x = __builtin_bit_cast(bf16x2, xu);
      a[2 * q] = __builtin_amdgcn_fdot2_f32_bf16(x, e0, a[2 * q], false);
      a[2 * q + 1] = __builtin_amdgcn_fdot2_f32_bf16(x, e1, a[2 * q + 1], false);
    }
  }
}
__device__ __forceinline__ void role_red(const Params& p, rsrc_t rs, LAS unsigned char* L, int t) {
  const int tid = threadIdx.x, lane = tid & 63, w = __builtin_amdgcn_readfirstlane(tid >> 6);
  const int c = w >> 1, ffn = w & 1;
  for (int s = 0; s < NSEG; ++s) {
    float a[8];
    if (!ffn) {
      wait_cnt<POLL>(rs, cnt_off(K_ATT, c, 0), NATT, t & 15, (unsigned)(s + 1), lane);
      sum_rows<NATT>(rs, (unsigned)(OFF_PO + (size_t)s * NATT * 32 * KB) + (unsigned)(t * 1024 + lane * 16), a);
    } else {
      wait_cnt<POLL>(rs, cnt_off(K_FFN, c, 0), NFFN, t % NFFN, (unsigned)(s + 1), lane);
      sum_rows<NFFN>(rs, (unsigned)(OFF_PF + (size_t)s * NFFN * 32 * KB) + (unsigned)(t * 1024 + lane * 16), a);
    }
    v4u xo;
    if (w == 0) xo = *(const v4u*)(p.x + (size_t)(s * SEG + t) * DM + lane * 8);
    else xo = *(volatile LAS v4u*)(L + s * 1024 + lane * 16);
    float x[8] = {bflo(xo.x), bfhi(xo.x), bflo(xo.y), bfhi(xo.y), bflo(xo.z), bfhi(xo.z), bflo(xo.w), bfhi(xo.w)};
#pragma unroll
    for (int j = 0; j < 8; ++j) x[j] = bf16r(x[j] + bf16r(a[j]));
    v4u xn; xn.x = pack2(x[0], x[1]); xn.y = pack2(x[2], x[3]); xn.z = pack2(x[4], x[5]); xn.w = pack2(x[6], x[7]);
    if (w == 3) {
      *(v4u*)(p.out + (size_t)(s * SEG + t) * DM + lane * 8) = xn;
    } else {
      *(volatile LAS v4u*)(L + s * 1024 + lane * 16) = xn;
      float y[8];
      layernorm8(x, y);
      v4u o; o.x = pack2(y[0], y[1]); o.y = pack2(y[2], y[3]); o.z = pack2(y[4], y[5]); o.w = pack2(y[6], y[7]);
      const unsigned dst = (ffn ? (unsigned)(OFF_XA + 2 * MB) : (unsigned)(OFF_XF + (size_t)c * 2 * MB)) + (unsigned)((s * SEG + t) * 1024 + lane * 16);
      st16(rs, dst, o);
      LDSWAIT();
      DRAIN();
      const unsigned f = ffn ? cnt_off(K_X2, 0, t) : cnt_off(K_X1, c, t);
      if (lane == 0) st4(rs, f, (unsigned)(s + 1));
    }
  }
}

__global__ void __launch_bounds__(256, 1) k_main(Params p) {
  __shared__ __attribute__((aligned(16))) unsigned char lds[LDS_BYTES];
  LAS unsigned char* L = (LAS unsigned char*)lds;
  const rsrc_t rs = __builtin_amdgcn_make_buffer_rsrc((void*)p.ws, 0, (int)WS_BYTES, 0x00020000);
  const int b = blockIdx.x;
  if (b >= 192 && b < 220)
    for (int i = 0; i < LATE; ++i) __builtin_amdgcn_s_sleep(100);
  if (b < 64) role_chain(p, rs, L, b >> 5, b & 31);
  else if (b < 96) role_creduce(p, rs, L, (b - 64) >> 4, (b - 64) & 15);
  else if (b < 160) role_qkv(p, rs, L, (b - 96) >> 5, (b - 96) & 31);
  else if (b < 192) role_att(p, rs, L, (b - 160) >> 4, (b - 160) & 15);
  else if (b < 220) role_ffn(p, rs, L, (b - 192) / NFFN, (b - 192) % NFFN);
  else if (b < 252) role_red(p, rs, L, b - 220);
}

extern "C" void kernel_launch(void* const* d_in, const int* in_sizes, int n_in,
                              void* d_out, int out_size, void* d_ws, size_t ws_size,
                              hipStream_t stream) {
  PrepParams pp;
  pp.x = (const u16*)d_in[0];
  pp.wq = (const u16*)d_in[2];
  pp.wk = (const u16*)d_in[3];
  pp.wv = (const u16*)d_in[4];
  pp.wo = (const u16*)d_in[5];
  pp.ws = (unsigned char*)d_ws;
  k_prep<<<NJ_ALL, 256, 0, stream>>>(pp);
  Params p;
  p.x = (const u16*)d_in[0];
  p.mem0 = (const u16*)d_in[1];
  p.wg = (const u16*)d_in[8];
  p.wu = (const u16*)d_in[9];
  p.wd = (const u16*)d_in[10];
  p.out = (u16*)d_out;
  p.ws = (unsigned char*)d_ws;
  k_main<<<252, 256, 0, stream>>>(p);
}
```
